```python
import torch

def _softmax(x, axis=-1):
    return torch.softmax(x, dim=axis)

def _gelu(x):
    return torch.nn.functional.gelu(x, approximate='tanh')

def _silu(x):
    return torch.nn.functional.silu(x)

def _sigmoid(x):
    return torch.sigmoid(x)

def _layernorm(x, axis=-1, eps=1e-05):
    m = x.mean(dim=axis, keepdim=True)
    v = x.var(dim=axis, keepdim=True, unbiased=False)
    return (x - m) / torch.sqrt(v + eps)

def _scatter_add(tgt, idx, src, axis=0):
    return tgt.clone().index_add_(axis, idx, src)

def _segment_sum(src, seg, n):
    out = torch.zeros((n,) + src.shape[1:], dtype=src.dtype)
    return out.index_add_(0, seg, src)

def _pad(x, pw):
    flat = tuple((v for pair in reversed(pw) for v in pair))
    return torch.nn.functional.pad(x, flat)
kernel_name = 'standalone__video_timesformer__vision'

def setup_inputs():

    _w = lambda a: (lambda t: t.to(torch.bfloat16) if t.is_floating_point() else t)(torch.as_tensor(a))
    T = 8
    p = 16
    HW = 512 // p * p
    N = (HW // p) ** 2
    C = 192
    return {'x': _w((lambda *s: torch.randn(*s, dtype=torch.bfloat16) * 0.1)(1, T, N, C)), 'wqkv_t': _w((lambda *s: torch.randn(*s, dtype=torch.bfloat16) * 0.1)(C, 3 * C)), 'wo_t': _w((lambda *s: torch.randn(*s, dtype=torch.bfloat16) * 0.1)(C, C)), 'wqkv_s': _w((lambda *s: torch.randn(*s, dtype=torch.bfloat16) * 0.1)(C, 3 * C)), 'wo_s': _w((lambda *s: torch.randn(*s, dtype=torch.bfloat16) * 0.05)(C, C))}

def reference(x, wqkv_t, wo_t, wqkv_s, wo_s):
    B, T, N, C = x.shape
    scale = C ** (-0.5)
    xt = (lambda x, *d: x.permute(*d))(x, 0, 2, 1, 3).reshape(B * N, T, C)
    h = _layernorm(xt, axis=-1)
    qkv = h @ wqkv_t
    q, k, v = (qkv[..., :C].clone(), qkv[..., C:2 * C].clone(), qkv[..., 2 * C:].clone())
    a = _softmax(torch.einsum('btc,bsc->bts', q, k) * scale, axis=-1)
    xt = xt + torch.einsum('bts,bsc->btc', a, v) @ wo_t
    x1 = (lambda x, *d: x.permute(*d))(xt.reshape(B, N, T, C), 0, 2, 1, 3)
    xs = x1.reshape(B * T, N, C)
    h = _layernorm(xs, axis=-1)
    qkv = h @ wqkv_s
    q, k, v = (qkv[..., :C].clone(), qkv[..., C:2 * C].clone(), qkv[..., 2 * C:].clone())
    a = _softmax(torch.einsum('bnc,bmc->bnm', q, k) * scale, axis=-1)
    xs = xs + torch.einsum('bnm,bmc->bnc', a, v) @ wo_s
    return xs.reshape(B, T, N, C)
```

```cpp
#include <hip/hip_runtime.h>
#include <hip/hip_bf16.h>
#include <stdint.h>

typedef unsigned short u16;
static __device__ __forceinline__ float bf2f(u16 v){ return __uint_as_float(((unsigned)v)<<16); }
static __device__ __forceinline__ u16 f2bf(float f){ __hip_bfloat16 b = __float2bfloat16(f); return *(u16*)&b; }
static __device__ __forceinline__ float rbf(float f){ return bf2f(f2bf(f)); }

constexpr int T=8,N=1024,C=192,M=T*N;

__global__ void ln_k(const float* x, float* h){
  int row = blockIdx.x; int l = threadIdx.x;
  float v[3]; float s=0;
  for(int i=0;i<3;i++){ v[i]=x[row*C+l+64*i]; s+=v[i]; }
  for(int o=32;o;o>>=1) s+=__shfl_xor(s,o);
  float m=s/C; float q=0;
  for(int i=0;i<3;i++){ float d=v[i]-m; q+=d*d; }
  for(int o=32;o;o>>=1) q+=__shfl_xor(q,o);
  float r=rsqrtf(q/C+1e-5f);
  for(int i=0;i<3;i++) h[row*C+l+64*i]=rbf((v[i]-m)*r);
}
__global__ void cvt_k(const u16* x, float* y, int n){ int i=blockIdx.x*256+threadIdx.x; if(i<n) y[i]=bf2f(x[i]); }
__global__ void gemm_k(const float* a, const u16* w, int Nn, float* out, const float* res, u16* outb){
  int row=blockIdx.y; int col=blockIdx.x*64+threadIdx.x;
  float s=0; for(int k=0;k<C;k++) s+=a[row*C+k]*bf2f(w[k*Nn+col]);
  s=rbf(s);
  if(res) s=rbf(s+res[row*Nn+col]);
  if(out) out[row*Nn+col]=s;
  if(outb) outb[row*Nn+col]=f2bf(s);
}
__global__ void attn_k(const float* qkv, float* o, int temporal){
  __shared__ float p[1024]; __shared__ float red[256];
  int row=blockIdx.x; int t=row/N, n=row%N; int tid=threadIdx.x;
  int nk = temporal? T: N; int kbase = temporal? n : t*N; int kstride = temporal? N : 1;
  const float* q=qkv+row*3*C;
  float mx=-1e30f;
  for(int j=tid;j<nk;j+=256){ const float* k=qkv+(kbase+j*kstride)*3*C+C; float s=0; for(int c=0;c<C;c++) s+=q[c]*k[c]; s*=0.07216878364870322f; p[j]=s; mx=fmaxf(mx,s);}
  red[tid]=mx; __syncthreads();
  for(int o2=128;o2;o2>>=1){ if(tid<o2) red[tid]=fmaxf(red[tid],red[tid+o2]); __syncthreads(); }
  mx=red[0]; __syncthreads();
  float sum=0; for(int j=tid;j<nk;j+=256){ float e=__expf(p[j]-mx); p[j]=e; sum+=e; }
  red[tid]=sum; __syncthreads();
  for(int o2=128;o2;o2>>=1){ if(tid<o2) red[tid]+=red[tid+o2]; __syncthreads(); }
  sum=red[0];
  if(tid<C){ float s=0; for(int j=0;j<nk;j++) s+=p[j]*qkv[(kbase+j*kstride)*3*C+2*C+tid]; o[row*C+tid]=rbf(s/sum); }
}

extern "C" void kernel_launch(void* const* d_in, const int* in_sizes, int n_in,
                              void* d_out, int out_size, void* d_ws, size_t ws_size,
                              hipStream_t stream) {
  const u16* x=(const u16*)d_in[0]; const u16* wqkv_t=(const u16*)d_in[1]; const u16* wo_t=(const u16*)d_in[2];
  const u16* wqkv_s=(const u16*)d_in[3]; const u16* wo_s=(const u16*)d_in[4];
  float* ws=(float*)d_ws;
  float* xf=ws; float* h=xf+M*C; float* qkv=h+M*C; float* o=qkv+M*3*C; float* x1=o+M*C;
  cvt_k<<<M*C/256,256,0,stream>>>(x,xf,M*C);
  ln_k<<<M,64,0,stream>>>(xf,h);
  gemm_k<<<dim3(9,M),64,0,stream>>>(h,wqkv_t,3*C,qkv,nullptr,nullptr);
  attn_k<<<M,256,0,stream>>>(qkv,o,1);
  gemm_k<<<dim3(3,M),64,0,stream>>>(o,wo_t,C,x1,xf,nullptr);
  ln_k<<<M,64,0,stream>>>(x1,h);
  gemm_k<<<dim3(9,M),64,0,stream>>>(h,wqkv_s,3*C,qkv,nullptr,nullptr);
  attn_k<<<M,256,0,stream>>>(qkv,o,0);
  gemm_k<<<dim3(3,M),64,0,stream>>>(o,wo_s,C,nullptr,x1,(u16*)d_out);
}
```

```cpp
#include <hip/hip_runtime.h>
#include <stdint.h>

typedef unsigned short u16;
typedef __bf16 bf16x8 __attribute__((ext_vector_type(8)));
typedef __bf16 bf16x2 __attribute__((ext_vector_type(2)));
typedef float f32x2 __attribute__((ext_vector_type(2)));
typedef float f32x4 __attribute__((ext_vector_type(4)));
typedef float f32x16 __attribute__((ext_vector_type(16)));
typedef unsigned u32x2 __attribute__((ext_vector_type(2)));
typedef unsigned u32x3 __attribute__((ext_vector_type(3)));
typedef unsigned u32x4 __attribute__((ext_vector_type(4)));
typedef short s16x4 __attribute__((ext_vector_type(4)));
struct __attribute__((packed, aligned(4))) U3 { unsigned a, b, c; };
#define LDSP __attribute__((address_space(3)))
typedef LDSP char* lptr;
#define DI __device__ __forceinline__

#define STAMPDECL int
#define STAMPARG 0
#define STAMPINIT()
#define STAMP(i)
#define STAMPF(i)
#define STAMPEND(k)

constexpr float kAlpha = 0.07216878364870322f * 1.4426950408889634f;
constexpr unsigned WS_H2 = 0, WS_MS = 3145728, WS_WVO = WS_MS + 73728;

DI unsigned cvtpk(float lo, float hi) { f32x2 v = {lo, hi}; bf16x2 b = __builtin_convertvector(v, bf16x2); return __builtin_bit_cast(unsigned, b); }
DI float bflo(unsigned u) { return __uint_as_float(u << 16); }
DI float bfhi(unsigned u) { return __uint_as_float(u & 0xffff0000u); }
DI u32x4 r128(lptr p) { return *(LDSP const u32x4*)p; }
DI void w128(lptr p, u32x4 v) { *(LDSP u32x4*)p = v; }
DI void w64(lptr p, u32x2 v) { *(LDSP u32x2*)p = v; }
DI u32x2 trd(lptr p) { return __builtin_bit_cast(u32x2, __builtin_amdgcn_ds_read_tr16_b64_v4i16((LDSP s16x4*)p)); }
DI u32x4 trfrag(lptr p, int second) { u32x2 a = trd(p), b = trd(p + second); return u32x4{a[0], a[1], b[0], b[1]}; }
DI f32x16 mfma32(u32x4 a, u32x4 b, f32x16 c) { return __builtin_amdgcn_mfma_f32_32x32x16_bf16(__builtin_bit_cast(bf16x8, a), __builtin_bit_cast(bf16x8, b), c, 0, 0, 0); }
DI f32x4 mfma16(u32x4 a, u32x4 b, f32x4 c) { return __builtin_amdgcn_mfma_f32_16x16x32_bf16(__builtin_bit_cast(bf16x8, a), __builtin_bit_cast(bf16x8, b), c, 0, 0, 0); }
DI float xsum32(float v) { auto r = __builtin_amdgcn_permlane32_swap(__float_as_uint(v), __float_as_uint(v), false, false); return __uint_as_float(r[0]) + __uint_as_float(r[1]); }
DI float xmax32(float v) { auto r = __builtin_amdgcn_permlane32_swap(__float_as_uint(v), __float_as_uint(v), false, false); return fmaxf(__uint_as_float(r[0]), __uint_as_float(r[1])); }
DI u32x4 pack8(const f32x16& a, int s) { return u32x4{cvtpk(a[8 * s], a[8 * s + 1]), cvtpk(a[8 * s + 2], a[8 * s + 3]), cvtpk(a[8 * s + 4], a[8 * s + 5]), cvtpk(a[8 * s + 6], a[8 * s + 7])}; }
#define SB() __builtin_amdgcn_sched_barrier(0)
#define BAR() asm volatile("s_waitcnt lgkmcnt(0)\n\ts_barrier" ::: "memory")
#define VBAR(n) asm volatile("s_waitcnt vmcnt(" #n ") lgkmcnt(0)\n\ts_barrier" ::: "memory")
DI void dma4(const char* src, unsigned voff, unsigned dst) {
  asm volatile("s_mov_b32 m0, %2\n\ts_nop 0\n\tglobal_load_lds_dwordx4 %0, %1\n\tglobal_load_lds_dwordx4 %0, %1 offset:1024\n\t"
               "global_load_lds_dwordx4 %0, %1 offset:2048\n\tglobal_load_lds_dwordx4 %0, %1 offset:3072" ::"v"(voff), "s"(src), "s"(dst) : "memory");
}
DI void dma3(const char* src, unsigned voff, unsigned dst) {
  asm volatile("s_mov_b32 m0, %2\n\ts_nop 0\n\tglobal_load_lds_dwordx4 %0, %1\n\tglobal_load_lds_dwordx4 %0, %1 offset:1024\n\t"
               "global_load_lds_dwordx4 %0, %1 offset:2048" ::"v"(voff), "s"(src), "s"(dst) : "memory");
}
template <int CTRL> DI float dppf(float v) { return __uint_as_float(__builtin_amdgcn_update_dpp(0, __float_as_uint(v), CTRL, 0xf, 0xf, true)); }
DI float sum8(float v) { v += dppf<0xB1>(v); v += dppf<0x4E>(v); v += dppf<0x141>(v); return v; }
DI void dma2(const char* src, unsigned voff, unsigned dst) {
  asm volatile("s_mov_b32 m0, %2\n\ts_nop 0\n\tglobal_load_lds_dwordx4 %0, %1\n\tglobal_load_lds_dwordx4 %0, %1 offset:1024" ::"v"(voff), "s"(src), "s"(dst) : "memory");
}

DI void ln_part(const float (&v)[16], lptr st, int w) {
  float s = 0.f;
#pragma unroll
  for (int i = 0; i < 16; ++i) s += v[i];
  const float mu = xsum32(s) * (1.f / 32.f);
  float q = 0.f;
#pragma unroll
  for (int i = 0; i < 16; ++i) { const float d = v[i] - mu; q = fmaf(d, d, q); }
  q = xsum32(q);
  w64(st + w * 8, u32x2{__float_as_uint(mu), __float_as_uint(q)});
}
DI void ln_fin(lptr st, float& mean, float& rstd) {
  const f32x4 a = __builtin_bit_cast(f32x4, r128(st)), b = __builtin_bit_cast(f32x4, r128(st + 16)), c = __builtin_bit_cast(f32x4, r128(st + 32));
  mean = (a[0] + a[2] + b[0] + b[2] + c[0] + c[2]) * (1.f / 6.f);
  const float d0 = a[0] - mean, d1 = a[2] - mean, d2 = b[0] - mean, d3 = b[2] - mean, d4 = c[0] - mean, d5 = c[2] - mean;
  const float m2 = (a[1] + a[3] + b[1] + b[3] + c[1] + c[3]) + 32.f * (d0 * d0 + d1 * d1 + d2 * d2 + d3 * d3 + d4 * d4 + d5 * d5);
  rstd = __builtin_amdgcn_rsqf(m2 * (1.f / 192.f) + 1e-5f);
}

constexpr int K1_SLOT = 36864, K1_HF = 147456, K1_ST2 = 160256, K1_EXV = 0, K1_EXS = 12288;

DI void k1_loader(lptr sm, const char* wqkv_t, const char* wo_t, int lane, int j2, int rot, STAMPDECL) {
  const unsigned voff = lane * 16, half = j2 * 18432, l0 = (unsigned)(uintptr_t)sm + half;
#pragma unroll 1
  for (int j = 0; j < 8; ++j) {
    int cc = j + rot; if (cc >= 6) cc -= 6;
    const char* src = (j < 6 ? wqkv_t + cc * K1_SLOT : wo_t + (j - 6) * K1_SLOT) + half;
    const unsigned dst = l0 + (j & 3) * K1_SLOT;
    dma4(src, voff, dst); dma4(src + 4096, voff, dst + 4096); dma4(src + 8192, voff, dst + 8192); dma4(src + 12288, voff, dst + 12288);
    dma2(src + 16384, voff, dst + 16384);
    STAMPF(1 + 3 * j);
    if (j >= 2) { asm volatile("s_waitcnt vmcnt(18)" ::: "memory"); STAMPF(2 + 3 * j); VBAR(18); STAMPF(3 + 3 * j); }
  }
  VBAR(0);
}

DI void k1_job(const char* wqkv_s, const char* wo_s, char* ws, int lane, int b) {
  const int r16 = lane & 15, g4 = lane >> 4;
  if (b < 144) {
    const int kt = b / 12, ct = b % 12;
    const char* pa = wqkv_s + (16 * kt + r16) * 1152 + g4 * 16;
    const char* pb = wqkv_s + (16 * ct + r16) * 1152 + 384 + g4 * 16;
    u32x4 ja[6], jb[6];
#pragma unroll
    for (int s = 0; s < 6; ++s) { ja[s] = *(const u32x4*)(pa + s * 64); jb[s] = *(const u32x4*)(pb + s * 64); }
    f32x4 d = {};
#pragma unroll
    for (int s = 0; s < 6; ++s) d = mfma16(ja[s], jb[s], d);
    const unsigned off = WS_MS + ((ct * 6 + (kt >> 1)) * 64 + r16 + 16 * (2 * (kt & 1) + (g4 >> 1))) * 16 + (g4 & 1) * 8;
    *(u32x2*)(ws + off) = u32x2{cvtpk(d[0] * kAlpha, d[1] * kAlpha), cvtpk(d[2] * kAlpha, d[3] * kAlpha)};
  } else if (b < 216) {
    const int id = b - 144, dt = id / 6, cp = id % 6;
    const char* pa = wqkv_s + (16 * dt + r16) * 1152 + 768 + g4 * 16;
    const unsigned ob = (8 * g4) * 384 + cp * 64 + r16 * 4;
    u32x4 ja[6]; unsigned jb[48];
#pragma unroll
    for (int s = 0; s < 6; ++s) {
      ja[s] = *(const u32x4*)(pa + s * 64);
      const char* pb = wo_s + s * 32 * 384;
#pragma unroll
      for (int e = 0; e < 8; ++e) jb[8 * s + e] = *(const unsigned*)(pb + (ob + e * 384));
    }
    f32x4 de = {}, dd = {};
#pragma unroll
    for (int s = 0; s < 6; ++s) {
      u32x4 fe, fo;
#pragma unroll
      for (int e = 0; e < 4; ++e) {
        fe[e] = __builtin_amdgcn_perm(jb[8 * s + 2 * e + 1], jb[8 * s + 2 * e], 0x05040100u);
        fo[e] = __builtin_amdgcn_perm(jb[8 * s + 2 * e + 1], jb[8 * s + 2 * e], 0x07060302u);
      }
      de = mfma16(ja[s], fe, de); dd = mfma16(ja[s], fo, dd);
    }
#pragma unroll
    for (int par = 0; par < 2; ++par) {
      const int cch = 32 * cp + 2 * r16 + par, ww = cch / 48, rem = cch % 48, rho = 4 * (rem / 12) + (rem & 3), ti = (rem % 12) >> 2;
      const unsigned off = WS_WVO + (((dt >> 1) * 12 + 3 * ww + ti) * 64 + rho + 16 * (2 * (dt & 1) + (g4 >> 1))) * 16 + (g4 & 1) * 8;
      const f32x4 d = par ? dd : de;
      *(u32x2*)(ws + off) = u32x2{cvtpk(d[0], d[1]), cvtpk(d[2], d[3])};
    }
  }
}

DI void k1_compute(lptr sm, const char* x, char* ws, char* x1out, int lane, int w, int b, int rot, STAMPDECL) {
  asm volatile("" :: "s"(x), "s"(ws), "s"(x1out));
  const int tau = lane & 31, h = lane >> 5, nl = tau >> 3, tt = tau & 7;
  const unsigned rowoff = (tt * 1024 + 4 * b + nl) * 384 + (32 * w + 8 * h) * 2;
  if (w < 4) {
    const int t8 = lane >> 3, c8 = lane & 7;
    const unsigned o = (t8 * 1024 + 4 * b + w) * 384 + c8 * 16;
    u32x4 r[3];
#pragma unroll
    for (int j = 0; j < 3; ++j) r[j] = *(const u32x4*)(x + (o + j * 128));
    float v[24];
#pragma unroll
    for (int i = 0; i < 12; ++i) { v[2 * i] = bflo(r[i >> 2][i & 3]); v[2 * i + 1] = bfhi(r[i >> 2][i & 3]); }
    float s0 = v[0], s1 = v[1];
#pragma unroll
    for (int i = 2; i < 24; i += 2) { s0 += v[i]; s1 += v[i + 1]; }
    const float mean = sum8(s0 + s1) * (1.f / 192.f);
    float q0 = 0.f, q1 = 0.f;
#pragma unroll
    for (int i = 0; i < 24; i += 2) { v[i] -= mean; v[i + 1] -= mean; q0 = fmaf(v[i], v[i], q0); q1 = fmaf(v[i + 1], v[i + 1], q1); }
    const float rstd = __builtin_amdgcn_rsqf(sum8(q0 + q1) * (1.f / 192.f) + 1e-5f);
#pragma unroll
    for (int j = 0; j < 3; ++j) {
      u32x4 hq;
#pragma unroll
      for (int i = 0; i < 4; ++i) hq[i] = cvtpk(v[8 * j + 2 * i] * rstd, v[8 * j + 2 * i + 1] * rstd);
      w128(sm + K1_HF + (c8 + 8 * j) * 528 + (8 * w + t8) * 16, hq);
    }
  }
  const u32x4 xa = *(const u32x4*)(x + rowoff), xb = *(const u32x4*)(x + (rowoff + 32));
  STAMP(1);
  BAR();
  STAMP(2);
  const int blk = (lane >> 4) & 1, q4 = (lane >> 2) & 3, p4 = lane & 3, sig = ((p4 & 1) << 1) | (p4 >> 1);
  const int colb = (32 * w + 16 * blk + 4 * sig) * 2;
  f32x16 aq = {}, ak = {}, av = {};
#pragma unroll 1
  for (int c = 0; c < 6; ++c) {
    int cc = c + rot; if (cc >= 6) cc -= 6;
    const lptr wp = sm + (c & 3) * K1_SLOT + (8 * h + q4) * 1152 + colb;
    const lptr hp = sm + K1_HF + cc * 2112 + h * 528 + tau * 16;
    u32x4 hf[2], wf[2][3];
#pragma unroll
    for (int s = 0; s < 2; ++s) {
      hf[s] = r128(hp + s * 1056);
#pragma unroll
      for (int m = 0; m < 3; ++m) wf[s][m] = trfrag(wp + s * 18432 + m * 384, 4608);
    }
    SB();
#pragma unroll
    for (int s = 0; s < 2; ++s) { aq = mfma32(wf[s][0], hf[s], aq); ak = mfma32(wf[s][1], hf[s], ak); av = mfma32(wf[s][2], hf[s], av); }
    STAMPF(10 + 2 * c);
    if (c >= 1 && c < 5) { BAR(); STAMPF(11 + 2 * c); }
  }
  STAMP(3);
  {
    f32x16 sp = mfma32(pack8(ak, 0), pack8(aq, 0), f32x16{});
    sp = mfma32(pack8(ak, 1), pack8(aq, 1), sp);
    w128(sm + K1_EXV + (2 * w) * 1024 + lane * 16, pack8(av, 0));
    w128(sm + K1_EXV + (2 * w + 1) * 1024 + lane * 16, pack8(av, 1));
    f32x4 s4;
#pragma unroll
    for (int e = 0; e < 4; ++e) {
      float a0 = sp[e], a1 = sp[4 + e], a2 = sp[8 + e], a3 = sp[12 + e];
      asm("" : "+v"(a0), "+v"(a1), "+v"(a2), "+v"(a3));
      const float lo = (nl & 1) ? a1 : a0, hi = (nl & 1) ? a3 : a2;
      s4[e] = (nl & 2) ? hi : lo;
    }
    w128(sm + K1_EXS + w * 1024 + lane * 16, __builtin_bit_cast(u32x4, s4));
  }
  BAR();
  STAMP(4);
  u32x4 pf[2];
  {
    f32x4 s4 = __builtin_bit_cast(f32x4, r128(sm + K1_EXS + lane * 16));
#pragma unroll
    for (int j = 1; j < 6; ++j) s4 += __builtin_bit_cast(f32x4, r128(sm + K1_EXS + j * 1024 + lane * 16));
    const float mx = xmax32(fmaxf(fmaxf(s4[0], s4[1]), fmaxf(s4[2], s4[3])));
    float e[4];
#pragma unroll
    for (int i = 0; i < 4; ++i) e[i] = __builtin_amdgcn_exp2f((s4[i] - mx) * kAlpha);
    const float inv = __builtin_amdgcn_rcpf(xsum32((e[0] + e[1]) + (e[2] + e[3])));
    const unsigned p01 = cvtpk(e[0] * inv, e[1] * inv), p23 = cvtpk(e[2] * inv, e[3] * inv);
#pragma unroll
    for (int g = 0; g < 4; ++g) { pf[g >> 1][2 * (g & 1)] = (nl == g) ? p01 : 0u; pf[g >> 1][2 * (g & 1) + 1] = (nl == g) ? p23 : 0u; }
  }
  f32x16 vw = {};
#pragma unroll 1
  for (int hh = 0; hh < 2; ++hh) {
    const lptr wp = sm + (2 + hh) * K1_SLOT + (8 * h + q4) * 384 + colb;
    const lptr vp = sm + K1_EXV + hh * 6144 + lane * 16;
    u32x4 af[6], bf[6];
#pragma unroll
    for (int s = 0; s < 6; ++s) { af[s] = r128(vp + s * 1024); bf[s] = trfrag(wp + s * 6144, 1536); }
    SB();
#pragma unroll
    for (int s = 0; s < 6; ++s) vw = mfma32(af[s], bf[s], vw);
    if (hh == 0) BAR();
  }
  STAMP(5);
  f32x16 y = mfma32(pack8(vw, 0), pf[0], f32x16{});
  y = mfma32(pack8(vw, 1), pf[1], y);
  u32x4 x1p[2];
  float xv[16];
#pragma unroll
  for (int s = 0; s < 2; ++s)
#pragma unroll
    for (int i = 0; i < 4; ++i) {
      const unsigned xr = s ? xb[i] : xa[i];
      x1p[s][i] = cvtpk(bflo(xr) + y[8 * s + 2 * i], bfhi(xr) + y[8 * s + 2 * i + 1]);
      xv[8 * s + 2 * i] = bflo(x1p[s][i]); xv[8 * s + 2 * i + 1] = bfhi(x1p[s][i]);
    }
  ln_part(xv, sm + K1_ST2 + tau * 48, w);
  __builtin_nontemporal_store(x1p[0], (u32x4*)(x1out + rowoff));
  __builtin_nontemporal_store(x1p[1], (u32x4*)(x1out + (rowoff + 32)));
  BAR();
  STAMP(6);
  {
    float mean, rstd; ln_fin(sm + K1_ST2 + tau * 48, mean, rstd);
    const float nb = -mean * rstd;
    const int n = 4 * b + nl, row = n & 31;
    const unsigned base = WS_H2 + tt * 393216 + (n >> 5) * 12288 + (2 * w) * 1024 + (2 * row + (h ^ (row >> 4))) * 16;
#pragma unroll
    for (int s = 0; s < 2; ++s) {
      u32x4 o;
#pragma unroll
      for (int i = 0; i < 4; ++i) o[i] = cvtpk(fmaf(xv[8 * s + 2 * i], rstd, nb), fmaf(xv[8 * s + 2 * i + 1], rstd, nb));
      __builtin_nontemporal_store(o, (u32x4*)(ws + (base + s * 1024)));
    }
  }
}

extern "C" __global__ __launch_bounds__(576, 3) void k_temporal(const char* __restrict__ x, const char* __restrict__ wqkv_t, const char* __restrict__ wo_t,
                                                                const char* __restrict__ wqkv_s, const char* __restrict__ wo_s, char* __restrict__ ws,
                                                                char* __restrict__ x1out) {
  __shared__ __attribute__((aligned(1024))) char smem_raw[163840];
  const lptr sm = (lptr)smem_raw;
  const int lane = threadIdx.x & 63, w = __builtin_amdgcn_readfirstlane(threadIdx.x >> 6), b = blockIdx.x;
  const int rot = (b >> 3) % 6;
  STAMPINIT();
  if (w < 6) k1_compute(sm, x, ws, x1out, lane, w, b, rot, STAMPARG);
  else if (w < 8) k1_loader(sm, wqkv_t, wo_t, lane, w - 6, rot, STAMPARG);
  else k1_job(wqkv_s, wo_s, ws, lane, b);
  STAMPEND(0);
}

constexpr int K2_GRP = 49152, K2_P = 147456, K2_NRM = K2_P + 12288, K2_MX = K2_P + 12544;
constexpr int K2_W1 = 2 * K2_GRP, K2_W2 = 0, K2_L = 24576, K2_O = 32768;
constexpr float kNormLimit = 32.0f;

DI bool k2_unsafe(lptr sm, int lane) {
  const float nrm = *(LDSP const float*)(sm + K2_NRM + (lane & 31) * 4);
  return __any(!(nrm <= kNormLimit));
}

DI void k2_loader(lptr sm, const char* ws, int lane, int j2, int t, int qt, STAMPDECL) {
  const int rot = qt >> 2;
  const char* h2 = ws + WS_H2 + t * 393216;
  const unsigned voff = lane * 16, l0 = (unsigned)(uintptr_t)sm;
  auto half_tile = [&](const char* src, unsigned dst) { dma3(src, voff, dst); dma3(src + 3072, voff, dst + 3072); };
  auto issue = [&](const char* src, unsigned dst, int n12) {
    src += j2 * n12 * 12288; dst += l0 + j2 * n12 * 12288;
#pragma unroll 1
    for (int i = 0; i < n12; ++i) { dma4(src, voff, dst); dma4(src + 4096, voff, dst + 4096); dma4(src + 8192, voff, dst + 8192); src += 12288; dst += 12288; }
  };
  {
    const char* g0 = h2 + rot * K2_GRP + j2 * 6144;
#pragma unroll 1
    for (int i = 0; i < 4; ++i) {
      const int k = ((qt + i) & 3) * 12288;
      half_tile(g0 + k, l0 + j2 * 6144 + k);
      if (i == 1) VBAR(6);
    }
  }
  issue(h2 + ((rot + 1) & 7) * K2_GRP, K2_GRP, 2);
  VBAR(24);
  if (__builtin_expect(k2_unsafe(sm, lane), 0)) {
    int sl = 2;
#pragma unroll 1
    for (int r = 0; r < 8; ++r) {
      if (r <= 5) issue(h2 + ((rot + r + 2) & 7) * K2_GRP, sl * K2_GRP, 2);
      sl = (sl == 2) ? 0 : sl + 1;
      VBAR(0);
    }
    issue(h2 + rot * K2_GRP, 0, 2);
    issue(h2 + ((rot + 1) & 7) * K2_GRP, K2_GRP, 2);
    VBAR(24);
    BAR();
  }
  {
    const char* src = h2 + ((rot + 2) & 7) * K2_GRP;
    issue(src, 2 * K2_GRP, 1);
    BAR();
    issue(src + 24576, 2 * K2_GRP + 24576, 1);
    VBAR(24);
  }
  int sl = 0;
#pragma unroll 1
  for (int r = 1; r < 7; ++r) {
    issue(r < 6 ? h2 + ((rot + r + 2) & 7) * K2_GRP : ws + WS_WVO, sl * K2_GRP, 2);
    sl = (sl == 2) ? 0 : sl + 1;
    STAMPF(10 + 3 * r);
    asm volatile("s_waitcnt vmcnt(24)" ::: "memory");
    STAMPF(11 + 3 * r);
    VBAR(24);
    STAMPF(12 + 3 * r);
  }
  issue(ws + WS_WVO + K2_GRP, K2_W2, 1);
  VBAR(12);
  VBAR(0);
}

DI void k2_pv(lptr sm, int lane, int v, STAMPDECL) {
  const int r16 = lane & 15, g4 = lane >> 4, q4 = r16 >> 2, p4 = lane & 3;
  if (lane < 32) *(LDSP float*)(sm + K2_NRM + lane * 4) = 0.f;
  BAR();
  BAR();
  if (__builtin_expect(k2_unsafe(sm, lane), 0)) {
#pragma unroll 1
    for (int r = 0; r < 10; ++r) BAR();
  }
  const int vb = v * 3072 + (16 * (g4 >> 1) + 4 * (g4 & 1) + q4) * 32 + (((p4 >> 1) ^ (g4 >> 1)) * 16) + (p4 & 1) * 8;
  u32x4 vf[4][3];
  f32x4 o[3][2] = {};
#pragma unroll
  for (int k = 0; k < 4; ++k)
#pragma unroll
    for (int i = 0; i < 3; ++i) vf[k][i] = trfrag(sm + vb + k * 12288 + i * 1024, 256);
  BAR();
  BAR();
  int sl = 1;
#pragma unroll 1
  for (int r = 1; r < 8; ++r) {
    const lptr pp = sm + K2_P + ((r - 1) & 1) * 8192 + lane * 16;
    u32x4 pf[4][2];
#pragma unroll
    for (int k = 0; k < 4; ++k) { pf[k][0] = r128(pp + k * 2048); pf[k][1] = r128(pp + k * 2048 + 1024); }
    const lptr vp = sm + sl * K2_GRP + vb;
    SB();
#pragma unroll
    for (int k = 0; k < 4; ++k) {
#pragma unroll
      for (int i = 0; i < 3; ++i) {
        o[i][0] = mfma16(vf[k][i], pf[k][0], o[i][0]); o[i][1] = mfma16(vf[k][i], pf[k][1], o[i][1]);
        SB();
        vf[k][i] = trfrag(vp + k * 12288 + i * 1024, 256);
        SB();
      }
    }
    sl = (sl == 2) ? 0 : sl + 1;
    STAMPF(10 + 2 * r);
    BAR();
    STAMPF(11 + 2 * r);
  }
  STAMP(3);
  const lptr pp = sm + K2_P + 8192 + lane * 16;
  u32x4 pf[4][2], lp[2][2];
#pragma unroll
  for (int k = 0; k < 4; ++k) { pf[k][0] = r128(pp + k * 2048); pf[k][1] = r128(pp + k * 2048 + 1024); }
#pragma unroll
  for (int u = 0; u < 2; ++u) { lp[u][0] = r128(sm + K2_L + (16 * u + r16) * 32); lp[u][1] = r128(sm + K2_L + (16 * u + r16) * 32 + 16); }
  SB();
#pragma unroll
  for (int k = 0; k < 4; ++k)
#pragma unroll
    for (int i = 0; i < 3; ++i) { o[i][0] = mfma16(vf[k][i], pf[k][0], o[i][0]); o[i][1] = mfma16(vf[k][i], pf[k][1], o[i][1]); }
#pragma unroll
  for (int u = 0; u < 2; ++u) {
    const f32x4 a = __builtin_bit_cast(f32x4, lp[u][0]), c = __builtin_bit_cast(f32x4, lp[u][1]);
    const float inv = __builtin_amdgcn_rcpf(((a[0] + a[1]) + (a[2] + a[3])) + ((c[0] + c[1]) + (c[2] + c[3])));
#pragma unroll
    for (int i = 0; i < 3; ++i) {
      const int d0 = 48 * v + 16 * i + 4 * g4;
      w64(sm + K2_O + (((d0 >> 5) * 2 + u) * 64 + r16 + 16 * ((d0 >> 3) & 3)) * 16 + (d0 & 4) * 2,
          u32x2{cvtpk(o[i][u][0] * inv, o[i][u][1] * inv), cvtpk(o[i][u][2] * inv, o[i][u][3] * inv)});
    }
  }
  BAR();
}

DI void k2_qk(lptr sm, const char* ws, char* out, int lane, int w, int t, int qt, STAMPDECL) {
  asm volatile("" :: "s"(out), "s"(ws));
  const int r16 = lane & 15, g4 = lane >> 4;
  {
    u32x4 wf[3][6];
    const char* mp = ws + WS_MS + (3 * w) * 6144;
#pragma unroll
    for (int s = 0; s < 6; ++s)
#pragma unroll
      for (int i = 0; i < 3; ++i) wf[i][s] = *(const u32x4*)(mp + i * 6144 + s * 1024 + (unsigned)(lane * 16));
    BAR();
    STAMP(1);
    f32x4 ga[3][2] = {};
    const lptr hq = sm + (qt & 3) * 12288 + (g4 >> 1) * 1024 + r16 * 32;
    u32x4 hf[6][2];
#pragma unroll
    for (int s = 0; s < 6; ++s)
#pragma unroll
      for (int u = 0; u < 2; ++u) hf[s][u] = r128(hq + s * 2048 + u * 512 + ((g4 & 1) ^ u) * 16);
    SB();
#pragma unroll
    for (int s = 0; s < 6; ++s) {
#pragma unroll
      for (int i = 0; i < 3; ++i)
#pragma unroll
        for (int u = 0; u < 2; ++u) ga[i][u] = mfma16(wf[i][s], hf[s][u], ga[i][u]);
      SB();
    }
#pragma unroll
    for (int u = 0; u < 2; ++u) {
      float n2 = 0.f;
#pragma unroll
      for (int i = 0; i < 3; ++i) {
#pragma unroll
        for (int e = 0; e < 4; ++e) n2 = fmaf(ga[i][u][e], ga[i][u][e], n2);
        w64(sm + K2_P + (3 * w + i) * 1024 + ((g4 >> 1) * 32 + 16 * u + r16) * 16 + (g4 & 1) * 8,
            u32x2{cvtpk(ga[i][u][0], ga[i][u][1]), cvtpk(ga[i][u][2], ga[i][u][3])});
      }
      __hip_atomic_fetch_add((LDSP float*)(sm + K2_NRM + (16 * u + r16) * 4), n2, __ATOMIC_RELAXED, __HIP_MEMORY_SCOPE_WORKGROUP);
    }
  }
  BAR();
  STAMP(2);
  u32x4 gf[12];
#pragma unroll
  for (int s = 0; s < 12; ++s) gf[s] = r128(sm + K2_P + s * 1024 + lane * 16);
  const int r32 = lane & 31, h = lane >> 5;
  const int kb = w * 12288 + (2 * r32 + (h ^ (r32 >> 4))) * 16;
  float nm = 0.f;
  if (__builtin_expect(k2_unsafe(sm, lane), 0)) {
    float m = -INFINITY; int sl = 0;
#pragma unroll 1
    for (int r = 0; r < 8; ++r) {
      const lptr kp = sm + sl * K2_GRP + kb;
      f32x16 s = {};
#pragma unroll
      for (int k = 0; k < 12; ++k) s = mfma32(r128(kp + k * 1024), gf[k], s);
#pragma unroll
      for (int i = 0; i < 16; ++i) m = fmaxf(m, s[i]);
      sl = (sl == 2) ? 0 : sl + 1;
      BAR();
    }
    m = xmax32(m);
    *(LDSP float*)(sm + K2_MX + r32 * 16 + w * 4) = m;
    BAR();
    const f32x4 mm = __builtin_bit_cast(f32x4, r128(sm + K2_MX + r32 * 16));
    nm = -fmaxf(fmaxf(mm[0], mm[1]), fmaxf(mm[2], mm[3]));
    BAR();
  }
  f32x16 negm;
#pragma unroll
  for (int i = 0; i < 16; ++i) negm[i] = nm;
  float lsum = 0.f;
  int sl = 0;
  __builtin_amdgcn_s_setprio(1);
#pragma unroll 1
  for (int r = 0; r < 8; ++r) {
    const lptr kp = sm + sl * K2_GRP + kb;
    u32x4 kf[12];
#pragma unroll
    for (int k = 0; k < 12; ++k) kf[k] = r128(kp + k * 1024);
    SB();
    f32x16 s = negm;
#pragma unroll
    for (int k = 0; k < 12; ++k) s = mfma32(kf[k], gf[k], s);
    if (r == 0) BAR();
    STAMPF(10 + 3 * r);
#pragma unroll
    for (int i = 0; i < 16; ++i) s[i] = __builtin_amdgcn_exp2f(s[i]);
    const lptr pp = sm + K2_P + (r & 1) * 8192 + w * 2048 + ((r32 >> 4) * 4 + h) * 256 + r16 * 16;
    w128(pp, pack8(s, 0));
    w128(pp + 512, pack8(s, 1));
    float a0 = s[0] + s[1], a1 = s[2] + s[3];
#pragma unroll
    for (int i = 4; i < 16; i += 2) { a0 += s[i]; a1 += s[i + 1]; }
    lsum += a0 + a1;
    if (r == 7) *(LDSP float*)(sm + K2_L + r32 * 32 + (2 * w + h) * 4) = lsum;
    sl = (sl == 2) ? 0 : sl + 1;
    STAMPF(11 + 3 * r);
    BAR();
    STAMPF(12 + 3 * r);
  }
  STAMP(3);
  const unsigned orow = (t * 1024 + qt * 32 + r16) * 384 + (48 * w + 12 * g4) * 2;
  U3 xr[2][2];
#pragma unroll
  for (int u = 0; u < 2; ++u) { xr[u][0] = *(const U3*)(out + (orow + u * 6144)); xr[u][1] = *(const U3*)(out + (orow + u * 6144 + 12)); }
  u32x4 wf[6][3];
#pragma unroll
  for (int s = 0; s < 4; ++s)
#pragma unroll
    for (int i = 0; i < 3; ++i) wf[s][i] = r128(sm + K2_W1 + (s * 12 + 3 * w + i) * 1024 + lane * 16);
  BAR();
  STAMP(4);
  u32x4 of[6][2];
#pragma unroll
  for (int s = 0; s < 6; ++s)
#pragma unroll
    for (int u = 0; u < 2; ++u) of[s][u] = r128(sm + K2_O + (s * 2 + u) * 1024 + lane * 16);
#pragma unroll
  for (int s = 4; s < 6; ++s)
#pragma unroll
    for (int i = 0; i < 3; ++i) wf[s][i] = r128(sm + K2_W2 + ((s - 4) * 12 + 3 * w + i) * 1024 + lane * 16);
  SB();
  f32x4 y[3][2] = {};
#pragma unroll
  for (int u = 0; u < 2; ++u)
#pragma unroll
    for (int s = 0; s < 6; ++s)
#pragma unroll
      for (int i = 0; i < 3; ++i) y[i][u] = mfma16(wf[s][i], of[s][u], y[i][u]);
#pragma unroll
  for (int u = 0; u < 2; ++u) {
    const unsigned xx[6] = {xr[u][0].a, xr[u][0].b, xr[u][0].c, xr[u][1].a, xr[u][1].b, xr[u][1].c};
    unsigned oo[6];
#pragma unroll
    for (int i = 0; i < 3; ++i) {
      oo[2 * i] = cvtpk(bflo(xx[2 * i]) + y[i][u][0], bfhi(xx[2 * i]) + y[i][u][1]);
      oo[2 * i + 1] = cvtpk(bflo(xx[2 * i + 1]) + y[i][u][2], bfhi(xx[2 * i + 1]) + y[i][u][3]);
    }
    *(U3*)(out + (orow + u * 6144)) = U3{oo[0], oo[1], oo[2]};
    *(U3*)(out + (orow + u * 6144 + 12)) = U3{oo[3], oo[4], oo[5]};
  }
}

extern "C" __global__ __launch_bounds__(640, 3) void k_spatial(const char* __restrict__ ws, char* __restrict__ out) {
  __shared__ __attribute__((aligned(1024))) char smem_raw[163840];
  const lptr sm = (lptr)smem_raw;
  const int lane = threadIdx.x & 63, w = __builtin_amdgcn_readfirstlane(threadIdx.x >> 6);
  const int t = blockIdx.x & 7, qt = blockIdx.x >> 3;
  STAMPINIT();
  if (w < 4) k2_qk(sm, ws, out, lane, w, t, qt, STAMPARG);
  else if (w < 8) k2_pv(sm, lane, w - 4, STAMPARG);
  else k2_loader(sm, ws, lane, w - 8, t, qt, STAMPARG);
  STAMPEND(1);
}

extern "C" void kernel_launch(void* const* d_in, const int* in_sizes, int n_in, void* d_out, int out_size, void* d_ws, size_t ws_size,
                              hipStream_t stream) {
  k_temporal<<<256, 576, 0, stream>>>((const char*)d_in[0], (const char*)d_in[1], (const char*)d_in[2], (const char*)d_in[3],
                                      (const char*)d_in[4], (char*)d_ws, (char*)d_out);
  k_spatial<<<256, 640, 0, stream>>>((const char*)d_ws, (char*)d_out);
}
```

```cpp
#include <hip/hip_runtime.h>
#include <stdint.h>

typedef unsigned u32x4 __attribute__((ext_vector_type(4)));
typedef unsigned u32x8 __attribute__((ext_vector_type(8)));
typedef unsigned u32x32 __attribute__((ext_vector_type(32)));
typedef __amdgpu_buffer_rsrc_t rsrc_t;
typedef __attribute__((address_space(3))) char* lptr;
#define DI __device__ __forceinline__
constexpr unsigned WS_H2 = 0, WS_WVT = 3145728, WS_MS = WS_WVT + 73728, WS_WVS = WS_MS + 73728, WS_ST = WS_WVS + 73728,
                   WS_FD = WS_ST + 65536, WS_FJ = WS_FD + 32768, WS_X = WS_FJ + 32768, WS_END = WS_X + 128;
constexpr int B_GRP = 49152, B_G = 147456, B_SEL = B_G + 12288, B_NRM = B_SEL + 2048;
constexpr float kNormLimit = 32.0f;

DI unsigned long long uni64(unsigned long long v) {
  return ((unsigned long long)(unsigned)__builtin_amdgcn_readfirstlane((unsigned)(v >> 32)) << 32) | (unsigned)__builtin_amdgcn_readfirstlane((unsigned)v);
}
DI unsigned tag0(unsigned x) { return x * 0x9E3779B1u + 0x7F4A7C15u; }
DI unsigned tag1(unsigned x) { return tag0(x) ^ 0x5bd1e995u; }

DI void p2_loader(unsigned long long wsp, char* out, unsigned ex, int lane, int j, int b) {
  const int t = b & 7, qt = b >> 3, r16 = lane & 15, g4 = lane >> 4, h = lane >> 5, u2 = j & 1, s2 = 4 + (j >> 1);
  const unsigned tb = WS_H2 + t * 393216 + qt * 12288 + h * 1024 + (r16 ^ (h * 4)) * 32 + (g4 & 1) * 16;
  const int n = qt * 32 + r16;
  const unsigned ob = (t * 1024 + n) * 384 + g4 * 16;
  u32x8 ra = {(unsigned)lane * 16, tb + j * 2048, ((tb + j * 2048) ^ 16) + 512, ((tb + s2 * 2048) ^ (u2 * 16)) + u2 * 512,
              WS_ST + (n >> 2) * 256 + ((n & 3) * 8 + t) * 8, (unsigned)(B_G + h * 1024 + ((g4 & 1) * 32 + r16) * 16), ob + j * 64, ob + s2 * 64 + u2 * 6144};
  unsigned nb = B_NRM + (lane & 31) * 64 + (lane & 32);
  asm volatile(
      "s_and_b32 s71, %[b], 7\n\t"
      "s_mul_i32 s71, s71, 0x60000\n\t"
      "s_add_u32 s64, %[wl], s71\n\t"
      "s_addc_u32 s65, %[wh], 0\n\t"
      "s_lshr_b32 s66, %[b], 5\n\t"
      "s_and_b32 s71, s66, 7\n\t"
      "s_mul_i32 s71, s71, 0xc000\n\t"
      "s_add_u32 s68, s64, s71\n\t"
      "s_addc_u32 s69, s65, 0\n\t"
      "s_mul_i32 s72, %[j], 0xc00\n\t"
      "s_add_u32 s78, s68, s72\n\t"
      "s_addc_u32 s79, s69, 0\n\t"
      "s_mul_i32 s67, %[j], 0x3000\n\t"
      "s_add_u32 s64, s64, s67\n\t"
      "s_addc_u32 s65, s65, 0\n\t"
      "s_lshr_b32 s73, %[b], 3\n\t"
      "s_mul_i32 s70, %[ex], 0x9E3779B1\n\t"
      "s_add_u32 s70, s70, 0x7F4A7C15\n\t"
      "s_xor_b32 s71, s70, 0x5bd1e995\n\t"
      "v_mov_b32 v12, s70\n\t"
      "v_mov_b32 v13, s71\n\t"
      "v_mov_b32 v14, s70\n\t"
      "v_mov_b32 v15, s71\n\t"
      "s_lshl_b32 s70, %[b], 7\n\t"
      "s_add_u32 s70, s70, 3432448\n\t"
      "s_add_u32 s68, %[wl], s70\n\t"
      "s_addc_u32 s69, %[wh], 0\n\t"
      "s_cmp_lg_u32 %[j], 2\n\t"
      "s_waitcnt vmcnt(0) lgkmcnt(0)\n\t"
      "s_barrier\n\t"
      "s_cbranch_scc1 95f\n\t"
      "s_mov_b64 exec, 0xff\n\t"
      "global_store_dwordx4 v0, v[12:15], s[68:69] sc1\n\t"
      "s_mov_b64 exec, -1\n\t"
      "95:\n\t"
      "s_and_b32 s71, s73, 3\n\t"
      "s_mul_i32 s71, s71, 0x3000\n\t"
      "s_add_u32 s68, s78, s71\n\t"
      "s_addc_u32 s69, s79, 0\n\t"
      "s_add_u32 m0, s71, s72\n\t"
      "s_add_u32 s73, s73, 1\n\t"
      "s_barrier\n\t"
      "global_load_lds_dwordx4 v0, s[68:69] offset:0 sc1\n\t"
      "global_load_lds_dwordx4 v0, s[68:69] offset:1024 sc1\n\t"
      "global_load_lds_dwordx4 v0, s[68:69] offset:2048 sc1\n\t"
      "s_and_b32 s71, s73, 3\n\t"
      "s_mul_i32 s71, s71, 0x3000\n\t"
      "s_add_u32 s68, s78, s71\n\t"
      "s_addc_u32 s69, s79, 0\n\t"
      "s_add_u32 m0, s71, s72\n\t"
      "s_add_u32 s73, s73, 1\n\t"
      "global_load_lds_dwordx4 v0, s[68:69] offset:0 sc1\n\t"
      "global_load_lds_dwordx4 v0, s[68:69] offset:1024 sc1\n\t"
      "global_load_lds_dwordx4 v0, s[68:69] offset:2048 sc1\n\t"
      "s_and_b32 s71, s73, 3\n\t"
      "s_mul_i32 s71, s71, 0x3000\n\t"
      "s_add_u32 s68, s78, s71\n\t"
      "s_addc_u32 s69, s79, 0\n\t"
      "s_add_u32 m0, s71, s72\n\t"
      "s_add_u32 s73, s73, 1\n\t"
      "global_load_lds_dwordx4 v0, s[68:69] offset:0 sc1\n\t"
      "global_load_lds_dwordx4 v0, s[68:69] offset:1024 sc1\n\t"
      "global_load_lds_dwordx4 v0, s[68:69] offset:2048 sc1\n\t"
      "s_waitcnt vmcnt(6)\n\t"
      "s_barrier\n\t"
      "s_and_b32 s71, s73, 3\n\t"
      "s_mul_i32 s71, s71, 0x3000\n\t"
      "s_add_u32 s68, s78, s71\n\t"
      "s_addc_u32 s69, s79, 0\n\t"
      "s_add_u32 m0, s71, s72\n\t"
      "s_add_u32 s73, s73, 1\n\t"
      "global_load_lds_dwordx4 v0, s[68:69] offset:0 sc1\n\t"
      "global_load_lds_dwordx4 v0, s[68:69] offset:1024 sc1\n\t"
      "global_load_lds_dwordx4 v0, s[68:69] offset:2048 sc1\n\t"
      "s_add_u32 s66, s66, 1\n\t"
      "s_add_u32 s67, s67, 49152\n\t"
      "s_cmp_ge_u32 s67, 147456\n\t"
      "s_cselect_b32 s71, 147456, 0\n\t"
      "s_sub_u32 s67, s67, s71\n\t"
      "s_and_b32 s71, s66, 7\n\t"
      "s_mul_i32 s71, s71, 0xc000\n\t"
      "s_add_u32 s68, s64, s71\n\t"
      "s_addc_u32 s69, s65, 0\n\t"
      "s_add_u32 s68, s68, 0xc00\n\t"
      "s_addc_u32 s69, s69, 0\n\t"
      "s_add_u32 m0, s67, 0xc00\n\t"
      "s_waitcnt vmcnt(0)\n\t"
      "s_barrier\n\t"
      "s_nop 0\n\t"
      "global_load_lds_dwordx4 v0, s[68:69] offset:-3072 sc1\n\t"
      "global_load_lds_dwordx4 v0, s[68:69] offset:-2048 sc1\n\t"
      "global_load_lds_dwordx4 v0, s[68:69] offset:-1024 sc1\n\t"
      "global_load_lds_dwordx4 v0, s[68:69] offset:0 sc1\n\t"
      "global_load_lds_dwordx4 v0, s[68:69] offset:1024 sc1\n\t"
      "global_load_lds_dwordx4 v0, s[68:69] offset:2048 sc1\n\t"
      "ds_read_b128 v[12:15], v8\n\t"
      "ds_read_b128 v[16:19], v8 offset:16\n\t"
      "s_add_u32 s68, s68, 0x1800\n\t"
      "s_addc_u32 s69, s69, 0\n\t"
      "s_add_u32 m0, s67, 0x2400\n\t"
      "s_nop 0\n\t"
      "global_load_lds_dwordx4 v0, s[68:69] offset:-3072 sc1\n\t"
      "global_load_lds_dwordx4 v0, s[68:69] offset:-2048 sc1\n\t"
      "global_load_lds_dwordx4 v0, s[68:69] offset:-1024 sc1\n\t"
      "global_load_lds_dwordx4 v0, s[68:69] offset:0 sc1\n\t"
      "global_load_lds_dwordx4 v0, s[68:69] offset:1024 sc1\n\t"
      "global_load_lds_dwordx4 v0, s[68:69] offset:2048 sc1\n\t"
      "s_add_u32 s66, s66, 1\n\t"
      "s_add_u32 s67, s67, 49152\n\t"
      "s_cmp_ge_u32 s67, 147456\n\t"
      "s_cselect_b32 s71, 147456, 0\n\t"
      "s_sub_u32 s67, s67, s71\n\t"
      "s_waitcnt lgkmcnt(0)\n\t"
      "v_add_f32 v12, v12, v13\n\t"
      "v_add_f32 v14, v14, v15\n\t"
      "v_add_f32 v16, v16, v17\n\t"
      "v_add_f32 v18, v18, v19\n\t"
      "v_add_f32 v12, v12, v14\n\t"
      "v_add_f32 v16, v16, v18\n\t"
      "v_add_f32 v12, v12, v16\n\t"
      "v_mov_b32 v13, v12\n\t"
      "s_nop 1\n\t"
      "v_permlane32_swap_b32 v12, v13\n\t"
      "v_add_f32 v12, v12, v13\n\t"
      "v_cmp_nge_f32 vcc, %[lim], v12\n\t"
      "s_mov_b32 s70, 5\n\t"
      "s_waitcnt vmcnt(6)\n\t"
      "s_barrier\n\t"
      "s_cbranch_vccnz 90f\n\t"
      "91:\n\t"
      "s_and_b32 s71, s66, 7\n\t"
      "s_mul_i32 s71, s71, 0xc000\n\t"
      "s_add_u32 s68, s64, s71\n\t"
      "s_addc_u32 s69, s65, 0\n\t"
      "s_add_u32 s68, s68, 0xc00\n\t"
      "s_addc_u32 s69, s69, 0\n\t"
      "s_add_u32 m0, s67, 0xc00\n\t"
      "s_nop 0\n\t"
      "global_load_lds_dwordx4 v0, s[68:69] offset:-3072 sc1\n\t"
      "global_load_lds_dwordx4 v0, s[68:69] offset:-2048 sc1\n\t"
      "global_load_lds_dwordx4 v0, s[68:69] offset:-1024 sc1\n\t"
      "global_load_lds_dwordx4 v0, s[68:69] offset:0 sc1\n\t"
      "global_load_lds_dwordx4 v0, s[68:69] offset:1024 sc1\n\t"
      "global_load_lds_dwordx4 v0, s[68:69] offset:2048 sc1\n\t"
      "s_waitcnt vmcnt(6)\n\t"
      "s_barrier\n\t"
      "s_add_u32 s68, s68, 0x1800\n\t"
      "s_addc_u32 s69, s69, 0\n\t"
      "s_add_u32 m0, s67, 0x2400\n\t"
      "s_nop 0\n\t"
      "global_load_lds_dwordx4 v0, s[68:69] offset:-3072 sc1\n\t"
      "global_load_lds_dwordx4 v0, s[68:69] offset:-2048 sc1\n\t"
      "global_load_lds_dwordx4 v0, s[68:69] offset:-1024 sc1\n\t"
      "global_load_lds_dwordx4 v0, s[68:69] offset:0 sc1\n\t"
      "global_load_lds_dwordx4 v0, s[68:69] offset:1024 sc1\n\t"
      "global_load_lds_dwordx4 v0, s[68:69] offset:2048 sc1\n\t"
      "s_add_u32 s66, s66, 1\n\t"
      "s_add_u32 s67, s67, 49152\n\t"
      "s_cmp_ge_u32 s67, 147456\n\t"
      "s_cselect_b32 s71, 147456, 0\n\t"
      "s_sub_u32 s67, s67, s71\n\t"
      "s_sub_u32 s70, s70, 1\n\t"
      "s_waitcnt vmcnt(6)\n\t"
      "s_barrier\n\t"
      "s_cbranch_scc0 91b\n\t"
      "s_mov_b32 s68, %[wl]\n\t"
      "s_mov_b32 s69, %[wh]\n\t"
      "global_load_dwordx4 v[12:15], v1, s[68:69] sc1\n\t"
      "global_load_dwordx4 v[16:19], v2, s[68:69] sc1\n\t"
      "global_load_dwordx4 v[20:23], v3, s[68:69] sc1\n\t"
      "global_load_dwordx2 v[24:25], v4, s[68:69] sc1\n\t"
      "global_load_dwordx2 v[26:27], v4, s[68:69] offset:1024 sc1\n\t"
      "s_mul_i32 s71, %[j], 0x3000\n\t"
      "s_add_u32 s71, s71, 3297280\n\t"
      "s_add_u32 s64, s68, s71\n\t"
      "s_addc_u32 s65, s69, 0\n\t"
      "s_add_u32 s66, s64, 0x2000\n\t"
      "s_addc_u32 s67, s65, 0\n\t"
      "s_lshr_b32 s71, %[j], 1\n\t"
      "s_mul_i32 s71, s71, 0x3000\n\t"
      "s_add_u32 s71, s71, 3346432\n\t"
      "s_add_u32 s72, s68, s71\n\t"
      "s_addc_u32 s73, s69, 0\n\t"
      "s_add_u32 s78, s72, 0x2000\n\t"
      "s_addc_u32 s79, s73, 0\n\t"
      "global_load_dwordx4 v[32:35], v0, s[64:65] offset:-4096 sc1\n\t"
      "s_waitcnt vmcnt(6)\n\t"
      "s_barrier\n\t"
      "global_load_dwordx4 v[36:39], v0, s[64:65] offset:-3072 sc1\n\t"
      "global_load_dwordx4 v[40:43], v0, s[64:65] offset:-2048 sc1\n\t"
      "global_load_dwordx4 v[44:47], v0, s[64:65] offset:-1024 sc1\n\t"
      "global_load_dwordx4 v[48:51], v0, s[64:65] offset:0 sc1\n\t"
      "global_load_dwordx4 v[52:55], v0, s[64:65] offset:1024 sc1\n\t"
      "global_load_dwordx4 v[56:59], v0, s[64:65] offset:2048 sc1\n\t"
      "global_load_dwordx4 v[60:63], v0, s[64:65] offset:3072 sc1\n\t"
      "s_barrier\n\t"
      "s_or_b32 s71, %[b], %[j]\n\t"
      "s_cbranch_scc1 92f\n\t"
      "s_add_u32 s71, %[ex], 1\n\t"
      "v_mov_b32 v9, s71\n\t"
      "v_mov_b32 v10, 3497984\n\t"
      "s_mov_b64 exec, 1\n\t"
      "global_store_dword v10, v9, s[68:69] sc1\n\t"
      "s_mov_b64 exec, -1\n\t"
      "92:\n\t"
      "global_load_dwordx4 v[64:67], v0, s[66:67] offset:-4096 sc1\n\t"
      "global_load_dwordx4 v[68:71], v0, s[66:67] offset:-3072 sc1\n\t"
      "global_load_dwordx4 v[72:75], v0, s[66:67] offset:-2048 sc1\n\t"
      "global_load_dwordx4 v[76:79], v0, s[66:67] offset:-1024 sc1\n\t"
      "global_load_dwordx4 v[80:83], v0, s[72:73] offset:-4096 sc1\n\t"
      "global_load_dwordx4 v[84:87], v0, s[72:73] offset:-3072 sc1\n\t"
      "global_load_dwordx4 v[88:91], v0, s[72:73] offset:-2048 sc1\n\t"
      "global_load_dwordx4 v[92:95], v0, s[72:73] offset:-1024 sc1\n\t"
      "global_load_dwordx4 v[96:99], v0, s[72:73] offset:0 sc1\n\t"
      "global_load_dwordx4 v[100:103], v0, s[72:73] offset:1024 sc1\n\t"
      "global_load_dwordx4 v[104:107], v0, s[72:73] offset:2048 sc1\n\t"
      "global_load_dwordx4 v[108:111], v0, s[72:73] offset:3072 sc1\n\t"
      "global_load_dwordx4 v[112:115], v0, s[78:79] offset:-4096 sc1\n\t"
      "global_load_dwordx4 v[116:119], v0, s[78:79] offset:-3072 sc1\n\t"
      "global_load_dwordx4 v[120:123], v0, s[78:79] offset:-2048 sc1\n\t"
      "global_load_dwordx4 v[124:127], v0, s[78:79] offset:-1024 sc1\n\t"
      "s_waitcnt vmcnt(24)\n\t"
      "s_bitcmp1_b32 %[j], 0\n\t"
      "s_cselect_b64 vcc, -1, 0\n\t"
      "s_mov_b32 s71, 0xffff0000\n\t"
      "v_cndmask_b32 v28, v24, v26, vcc\n\t"
      "v_cndmask_b32 v29, v25, v27, vcc\n\t"
      "v_lshlrev_b32 v128, 16, v12\n\t"
      "v_and_b32 v129, s71, v12\n\t"
      "v_lshlrev_b32 v130, 16, v13\n\t"
      "v_and_b32 v131, s71, v13\n\t"
      "v_lshlrev_b32 v132, 16, v14\n\t"
      "v_and_b32 v133, s71, v14\n\t"
      "v_lshlrev_b32 v134, 16, v15\n\t"
      "v_and_b32 v135, s71, v15\n\t"
      "v_fma_f32 v128, v128, v25, v24\n\t"
      "v_fma_f32 v129, v129, v25, v24\n\t"
      "v_fma_f32 v130, v130, v25, v24\n\t"
      "v_fma_f32 v131, v131, v25, v24\n\t"
      "v_fma_f32 v132, v132, v25, v24\n\t"
      "v_fma_f32 v133, v133, v25, v24\n\t"
      "v_fma_f32 v134, v134, v25, v24\n\t"
      "v_fma_f32 v135, v135, v25, v24\n\t"
      "v_lshlrev_b32 v136, 16, v16\n\t"
      "v_and_b32 v137, s71, v16\n\t"
      "v_lshlrev_b32 v138, 16, v17\n\t"
      "v_and_b32 v139, s71, v17\n\t"
      "v_lshlrev_b32 v140, 16, v18\n\t"
      "v_and_b32 v141, s71, v18\n\t"
      "v_lshlrev_b32 v142, 16, v19\n\t"
      "v_and_b32 v143, s71, v19\n\t"
      "v_fma_f32 v136, v136, v27, v26\n\t"
      "v_fma_f32 v137, v137, v27, v26\n\t"
      "v_fma_f32 v138, v138, v27, v26\n\t"
      "v_fma_f32 v139, v139, v27, v26\n\t"
      "v_fma_f32 v140, v140, v27, v26\n\t"
      "v_fma_f32 v141, v141, v27, v26\n\t"
      "v_fma_f32 v142, v142, v27, v26\n\t"
      "v_fma_f32 v143, v143, v27, v26\n\t"
      "v_lshlrev_b32 v144, 16, v20\n\t"
      "v_and_b32 v145, s71, v20\n\t"
      "v_lshlrev_b32 v146, 16, v21\n\t"
      "v_and_b32 v147, s71, v21\n\t"
      "v_lshlrev_b32 v148, 16, v22\n\t"
      "v_and_b32 v149, s71, v22\n\t"
      "v_lshlrev_b32 v150, 16, v23\n\t"
      "v_and_b32 v151, s71, v23\n\t"
      "v_fma_f32 v144, v144, v29, v28\n\t"
      "v_fma_f32 v145, v145, v29, v28\n\t"
      "v_fma_f32 v146, v146, v29, v28\n\t"
      "v_fma_f32 v147, v147, v29, v28\n\t"
      "v_fma_f32 v148, v148, v29, v28\n\t"
      "v_fma_f32 v149, v149, v29, v28\n\t"
      "v_fma_f32 v150, v150, v29, v28\n\t"
      "v_fma_f32 v151, v151, v29, v28\n\t"
      "s_cselect_b32 s71, 0x100, 0\n\t"
      "v_add_u32 v11, s71, v5\n\t"
      "s_barrier\n\t"
      "s_waitcnt vmcnt(0)\n\t"
      "s_barrier\n\t"
      "ds_read_b128 v[152:155], v5 offset:0\n\t"
      "ds_read_b128 v[156:159], v5 offset:2048\n\t"
      "ds_read_b128 v[160:163], v5 offset:4096\n\t"
      "ds_read_b128 v[164:167], v5 offset:6144\n\t"
      "ds_read_b128 v[168:171], v5 offset:8192\n\t"
      "ds_read_b128 v[172:175], v5 offset:10240\n\t"
      "ds_read_b128 v[176:179], v5 offset:256\n\t"
      "ds_read_b128 v[180:183], v5 offset:2304\n\t"
      "ds_read_b128 v[184:187], v5 offset:4352\n\t"
      "ds_read_b128 v[188:191], v5 offset:6400\n\t"
      "ds_read_b128 v[192:195], v5 offset:8448\n\t"
      "ds_read_b128 v[196:199], v5 offset:10496\n\t"
      "ds_read_b128 v[200:203], v11 offset:0\n\t"
      "ds_read_b128 v[204:207], v11 offset:2048\n\t"
      "ds_read_b128 v[208:211], v11 offset:4096\n\t"
      "ds_read_b128 v[212:215], v11 offset:6144\n\t"
      "ds_read_b128 v[216:219], v11 offset:8192\n\t"
      "ds_read_b128 v[220:223], v11 offset:10240\n\t"
      "s_waitcnt lgkmcnt(15)\n\t"
      "v_mfma_f32_16x16x32_bf16 v[128:131], v[32:35], v[152:155], v[128:131]\n\t"
      "v_mfma_f32_16x16x32_bf16 v[132:135], v[56:59], v[152:155], v[132:135]\n\t"
      "s_waitcnt lgkmcnt(15)\n\t"
      "v_mfma_f32_16x16x32_bf16 v[128:131], v[36:39], v[156:159], v[128:131]\n\t"
      "v_mfma_f32_16x16x32_bf16 v[132:135], v[60:63], v[156:159], v[132:135]\n\t"
      "s_waitcnt lgkmcnt(15)\n\t"
      "v_mfma_f32_16x16x32_bf16 v[128:131], v[40:43], v[160:163], v[128:131]\n\t"
      "v_mfma_f32_16x16x32_bf16 v[132:135], v[64:67], v[160:163], v[132:135]\n\t"
      "s_waitcnt lgkmcnt(14)\n\t"
      "v_mfma_f32_16x16x32_bf16 v[128:131], v[44:47], v[164:167], v[128:131]\n\t"
      "v_mfma_f32_16x16x32_bf16 v[132:135], v[68:71], v[164:167], v[132:135]\n\t"
      "s_waitcnt lgkmcnt(13)\n\t"
      "v_mfma_f32_16x16x32_bf16 v[128:131], v[48:51], v[168:171], v[128:131]\n\t"
      "v_mfma_f32_16x16x32_bf16 v[132:135], v[72:75], v[168:171], v[132:135]\n\t"
      "s_waitcnt lgkmcnt(12)\n\t"
      "v_mfma_f32_16x16x32_bf16 v[128:131], v[52:55], v[172:175], v[128:131]\n\t"
      "v_mfma_f32_16x16x32_bf16 v[132:135], v[76:79], v[172:175], v[132:135]\n\t"
      "s_waitcnt lgkmcnt(11)\n\t"
      "v_mfma_f32_16x16x32_bf16 v[136:139], v[32:35], v[176:179], v[136:139]\n\t"
      "v_mfma_f32_16x16x32_bf16 v[140:143], v[56:59], v[176:179], v[140:143]\n\t"
      "s_waitcnt lgkmcnt(10)\n\t"
      "v_mfma_f32_16x16x32_bf16 v[136:139], v[36:39], v[180:183], v[136:139]\n\t"
      "v_mfma_f32_16x16x32_bf16 v[140:143], v[60:63], v[180:183], v[140:143]\n\t"
      "s_waitcnt lgkmcnt(9)\n\t"
      "v_mfma_f32_16x16x32_bf16 v[136:139], v[40:43], v[184:187], v[136:139]\n\t"
      "v_mfma_f32_16x16x32_bf16 v[140:143], v[64:67], v[184:187], v[140:143]\n\t"
      "s_waitcnt lgkmcnt(8)\n\t"
      "v_mfma_f32_16x16x32_bf16 v[136:139], v[44:47], v[188:191], v[136:139]\n\t"
      "v_mfma_f32_16x16x32_bf16 v[140:143], v[68:71], v[188:191], v[140:143]\n\t"
      "v_cvt_pk_bf16_f32 v12, v128, v129\n\t"
      "v_cvt_pk_bf16_f32 v13, v130, v131\n\t"
      "s_waitcnt lgkmcnt(7)\n\t"
      "v_mfma_f32_16x16x32_bf16 v[136:139], v[48:51], v[192:195], v[136:139]\n\t"
      "v_mfma_f32_16x16x32_bf16 v[140:143], v[72:75], v[192:195], v[140:143]\n\t"
      "v_cvt_pk_bf16_f32 v14, v132, v133\n\t"
      "v_cvt_pk_bf16_f32 v15, v134, v135\n\t"
      "s_waitcnt lgkmcnt(6)\n\t"
      "v_mfma_f32_16x16x32_bf16 v[136:139], v[52:55], v[196:199], v[136:139]\n\t"
      "v_mfma_f32_16x16x32_bf16 v[140:143], v[76:79], v[196:199], v[140:143]\n\t"
      "global_store_dwordx4 v6, v[12:15], %[o0] sc1\n\t"
      "s_waitcnt lgkmcnt(5)\n\t"
      "v_mfma_f32_16x16x32_bf16 v[144:147], v[80:83], v[200:203], v[144:147]\n\t"
      "v_mfma_f32_16x16x32_bf16 v[148:151], v[104:107], v[200:203], v[148:151]\n\t"
      "s_waitcnt lgkmcnt(4)\n\t"
      "v_mfma_f32_16x16x32_bf16 v[144:147], v[84:87], v[204:207], v[144:147]\n\t"
      "v_mfma_f32_16x16x32_bf16 v[148:151], v[108:111], v[204:207], v[148:151]\n\t"
      "s_waitcnt lgkmcnt(3)\n\t"
      "v_mfma_f32_16x16x32_bf16 v[144:147], v[88:91], v[208:211], v[144:147]\n\t"
      "v_mfma_f32_16x16x32_bf16 v[148:151], v[112:115], v[208:211], v[148:151]\n\t"
      "s_waitcnt lgkmcnt(2)\n\t"
      "v_mfma_f32_16x16x32_bf16 v[144:147], v[92:95], v[212:215], v[144:147]\n\t"
      "v_mfma_f32_16x16x32_bf16 v[148:151], v[116:119], v[212:215], v[148:151]\n\t"
      "v_cvt_pk_bf16_f32 v16, v136, v137\n\t"
      "v_cvt_pk_bf16_f32 v17, v138, v139\n\t"
      "s_waitcnt lgkmcnt(1)\n\t"
      "v_mfma_f32_16x16x32_bf16 v[144:147], v[96:99], v[216:219], v[144:147]\n\t"
      "v_mfma_f32_16x16x32_bf16 v[148:151], v[120:123], v[216:219], v[148:151]\n\t"
      "v_cvt_pk_bf16_f32 v18, v140, v141\n\t"
      "v_cvt_pk_bf16_f32 v19, v142, v143\n\t"
      "s_waitcnt lgkmcnt(0)\n\t"
      "v_mfma_f32_16x16x32_bf16 v[144:147], v[100:103], v[220:223], v[144:147]\n\t"
      "v_mfma_f32_16x16x32_bf16 v[148:151], v[124:127], v[220:223], v[148:151]\n\t"
      "global_store_dwordx4 v6, v[16:19], %[o1] sc1\n\t"
      "s_nop 7\n\t"
      "v_cvt_pk_bf16_f32 v20, v144, v145\n\t"
      "v_cvt_pk_bf16_f32 v21, v146, v147\n\t"
      "v_cvt_pk_bf16_f32 v22, v148, v149\n\t"
      "v_cvt_pk_bf16_f32 v23, v150, v151\n\t"
      "global_store_dwordx4 v7, v[20:23], %[o0] sc1\n\t"
      "s_branch 99f\n\t"
      ".Lkf_last:\n\t"
      "90:\n\t"
      "s_mov_b32 s70, 7\n\t"
      "93:\n\t"
      "s_cmp_lt_u32 s70, 2\n\t"
      "s_cbranch_scc1 94f\n\t"
      "s_and_b32 s71, s66, 7\n\t"
      "s_mul_i32 s71, s71, 0xc000\n\t"
      "s_add_u32 s68, s64, s71\n\t"
      "s_addc_u32 s69, s65, 0\n\t"
      "s_add_u32 s68, s68, 0xc00\n\t"
      "s_addc_u32 s69, s69, 0\n\t"
      "s_add_u32 m0, s67, 0xc00\n\t"
      "s_nop 0\n\t"
      "global_load_lds_dwordx4 v0, s[68:69] offset:-3072 sc1\n\t"
      "global_load_lds_dwordx4 v0, s[68:69] offset:-2048 sc1\n\t"
      "global_load_lds_dwordx4 v0, s[68:69] offset:-1024 sc1\n\t"
      "global_load_lds_dwordx4 v0, s[68:69] offset:0 sc1\n\t"
      "global_load_lds_dwordx4 v0, s[68:69] offset:1024 sc1\n\t"
      "global_load_lds_dwordx4 v0, s[68:69] offset:2048 sc1\n\t"
      "s_add_u32 s68, s68, 0x1800\n\t"
      "s_addc_u32 s69, s69, 0\n\t"
      "s_add_u32 m0, s67, 0x2400\n\t"
      "s_nop 0\n\t"
      "global_load_lds_dwordx4 v0, s[68:69] offset:-3072 sc1\n\t"
      "global_load_lds_dwordx4 v0, s[68:69] offset:-2048 sc1\n\t"
      "global_load_lds_dwordx4 v0, s[68:69] offset:-1024 sc1\n\t"
      "global_load_lds_dwordx4 v0, s[68:69] offset:0 sc1\n\t"
      "global_load_lds_dwordx4 v0, s[68:69] offset:1024 sc1\n\t"
      "global_load_lds_dwordx4 v0, s[68:69] offset:2048 sc1\n\t"
      "s_add_u32 s66, s66, 1\n\t"
      "s_add_u32 s67, s67, 49152\n\t"
      "s_cmp_ge_u32 s67, 147456\n\t"
      "s_cselect_b32 s71, 147456, 0\n\t"
      "s_sub_u32 s67, s67, s71\n\t"
      "94:\n\t"
      "s_sub_u32 s70, s70, 1\n\t"
      "s_waitcnt vmcnt(0)\n\t"
      "s_barrier\n\t"
      "s_cbranch_scc0 93b\n\t"
      "s_mul_i32 s67, %[j], 0x3000\n\t"
      "s_and_b32 s71, s66, 7\n\t"
      "s_mul_i32 s71, s71, 0xc000\n\t"
      "s_add_u32 s68, s64, s71\n\t"
      "s_addc_u32 s69, s65, 0\n\t"
      "s_add_u32 s68, s68, 0xc00\n\t"
      "s_addc_u32 s69, s69, 0\n\t"
      "s_add_u32 m0, s67, 0xc00\n\t"
      "s_nop 0\n\t"
      "global_load_lds_dwordx4 v0, s[68:69] offset:-3072 sc1\n\t"
      "global_load_lds_dwordx4 v0, s[68:69] offset:-2048 sc1\n\t"
      "global_load_lds_dwordx4 v0, s[68:69] offset:-1024 sc1\n\t"
      "global_load_lds_dwordx4 v0, s[68:69] offset:0 sc1\n\t"
      "global_load_lds_dwordx4 v0, s[68:69] offset:1024 sc1\n\t"
      "global_load_lds_dwordx4 v0, s[68:69] offset:2048 sc1\n\t"
      "s_add_u32 s68, s68, 0x1800\n\t"
      "s_addc_u32 s69, s69, 0\n\t"
      "s_add_u32 m0, s67, 0x2400\n\t"
      "s_nop 0\n\t"
      "global_load_lds_dwordx4 v0, s[68:69] offset:-3072 sc1\n\t"
      "global_load_lds_dwordx4 v0, s[68:69] offset:-2048 sc1\n\t"
      "global_load_lds_dwordx4 v0, s[68:69] offset:-1024 sc1\n\t"
      "global_load_lds_dwordx4 v0, s[68:69] offset:0 sc1\n\t"
      "global_load_lds_dwordx4 v0, s[68:69] offset:1024 sc1\n\t"
      "global_load_lds_dwordx4 v0, s[68:69] offset:2048 sc1\n\t"
      "s_add_u32 s66, s66, 1\n\t"
      "s_add_u32 s67, s67, 49152\n\t"
      "s_cmp_ge_u32 s67, 147456\n\t"
      "s_cselect_b32 s71, 147456, 0\n\t"
      "s_sub_u32 s67, s67, s71\n\t"
      "s_waitcnt vmcnt(0)\n\t"
      "s_barrier\n\t"
      "s_and_b32 s71, s66, 7\n\t"
      "s_mul_i32 s71, s71, 0xc000\n\t"
      "s_add_u32 s68, s64, s71\n\t"
      "s_addc_u32 s69, s65, 0\n\t"
      "s_add_u32 s68, s68, 0xc00\n\t"
      "s_addc_u32 s69, s69, 0\n\t"
      "s_add_u32 m0, s67, 0xc00\n\t"
      "s_nop 0\n\t"
      "global_load_lds_dwordx4 v0, s[68:69] offset:-3072 sc1\n\t"
      "global_load_lds_dwordx4 v0, s[68:69] offset:-2048 sc1\n\t"
      "global_load_lds_dwordx4 v0, s[68:69] offset:-1024 sc1\n\t"
      "global_load_lds_dwordx4 v0, s[68:69] offset:0 sc1\n\t"
      "global_load_lds_dwordx4 v0, s[68:69] offset:1024 sc1\n\t"
      "global_load_lds_dwordx4 v0, s[68:69] offset:2048 sc1\n\t"
      "s_add_u32 s68, s68, 0x1800\n\t"
      "s_addc_u32 s69, s69, 0\n\t"
      "s_add_u32 m0, s67, 0x2400\n\t"
      "s_nop 0\n\t"
      "global_load_lds_dwordx4 v0, s[68:69] offset:-3072 sc1\n\t"
      "global_load_lds_dwordx4 v0, s[68:69] offset:-2048 sc1\n\t"
      "global_load_lds_dwordx4 v0, s[68:69] offset:-1024 sc1\n\t"
      "global_load_lds_dwordx4 v0, s[68:69] offset:0 sc1\n\t"
      "global_load_lds_dwordx4 v0, s[68:69] offset:1024 sc1\n\t"
      "global_load_lds_dwordx4 v0, s[68:69] offset:2048 sc1\n\t"
      "s_add_u32 s66, s66, 1\n\t"
      "s_add_u32 s67, s67, 49152\n\t"
      "s_cmp_ge_u32 s67, 147456\n\t"
      "s_cselect_b32 s71, 147456, 0\n\t"
      "s_sub_u32 s67, s67, s71\n\t"
      "s_mov_b32 s70, 5\n\t"
      "s_waitcnt vmcnt(6)\n\t"
      "s_barrier\n\t"
      "s_branch 91b\n\t"
      "99:\n\t"
      : "+{v[0:7]}"(ra), "+{v8}"(nb)
      : [wl] "s"((unsigned)wsp), [wh] "s"((unsigned)(wsp >> 32)), [o0] "s"(out), [o1] "s"(out + 6144), [b] "s"(b), [j] "s"(j), [ex] "s"(ex), [lim] "s"(kNormLimit)
      : "memory", "scc", "vcc", "s64", "s65", "s66", "s67", "s68", "s69", "s70", "s71", "s72", "s73", "s78", "s79", "v9", "v10", "v11", "v12", "v13", "v14", "v15", "v16", "v17", "v18", "v19", "v20", "v21", "v22", "v23", "v24", "v25", "v26", "v27", "v28", "v29", "v30", "v31", "v32", "v33", "v34", "v35", "v36", "v37", "v38", "v39", "v40", "v41", "v42", "v43", "v44", "v45", "v46", "v47", "v48", "v49", "v50", "v51", "v52", "v53", "v54", "v55", "v56", "v57", "v58", "v59", "v60", "v61", "v62", "v63", "v64", "v65", "v66", "v67", "v68", "v69", "v70", "v71", "v72", "v73", "v74", "v75", "v76", "v77", "v78", "v79", "v80", "v81", "v82", "v83", "v84", "v85", "v86", "v87", "v88", "v89", "v90", "v91", "v92", "v93", "v94", "v95", "v96", "v97", "v98", "v99", "v100", "v101", "v102", "v103", "v104", "v105", "v106", "v107", "v108", "v109", "v110", "v111", "v112", "v113", "v114", "v115", "v116", "v117", "v118", "v119", "v120", "v121", "v122", "v123", "v124", "v125", "v126", "v127", "v128", "v129", "v130", "v131", "v132", "v133", "v134", "v135", "v136", "v137", "v138", "v139", "v140", "v141", "v142", "v143", "v144", "v145", "v146", "v147", "v148", "v149", "v150", "v151", "v152", "v153", "v154", "v155", "v156", "v157", "v158", "v159", "v160", "v161", "v162", "v163", "v164", "v165", "v166", "v167", "v168", "v169", "v170", "v171", "v172", "v173", "v174", "v175", "v176", "v177", "v178", "v179", "v180", "v181", "v182", "v183", "v184", "v185", "v186", "v187", "v188", "v189", "v190", "v191", "v192", "v193", "v194", "v195", "v196", "v197", "v198", "v199", "v200", "v201", "v202", "v203", "v204", "v205", "v206", "v207", "v208", "v209", "v210", "v211", "v212", "v213", "v214", "v215", "v216", "v217", "v218", "v219", "v220", "v221", "v222", "v223");
}

DI void p2_attn(u32x32 m0, u32x32 m1, u32x8 m2, rsrc_t rs, unsigned ex, int lane, int w, int qt) {
  const int r16 = lane & 15, g4 = lane >> 4, r32 = lane & 31, h = lane >> 5;
  const int blk = g4 & 1, q4 = r16 >> 2, p4 = lane & 3;
  const int d = r32 - 16 * (w & 1) - 4 * h;
  const int je = (d >= 0 && d < 4) ? d : ((d >= 8 && d < 12) ? d - 4 : 8);
  const unsigned one = (je & 1) ? 0x3f800000u : 0x00003f80u;
  u32x4 sel = {(je >> 1) == 0 ? one : 0u, (je >> 1) == 1 ? one : 0u, (je >> 1) == 2 ? one : 0u, (je >> 1) == 3 ? one : 0u};
  const unsigned k0 = w * 12288 + (2 * r32 + (h ^ (r32 >> 4))) * 16;
  const unsigned v0 = w * 12288 + blk * 1024 + (4 * (h ^ blk) + q4) * 32 + (p4 >> 1) * 16 + (p4 & 1) * 8;
  const unsigned la = lane * 16, hq = (qt & 3) * 12288 + h * 1024 + (r16 ^ (h * 4)) * 32 + blk * 16;
  u32x8 ra = {0u, k0, k0 ^ 128, v0, (v0 ^ 16) + 512, la, B_SEL + (w & 1) * 1024 + la, WS_FD + ((qt >> 2) * 32 + r32) * 128};
  u32x8 rb = {B_G + la, 0u, hq, (hq ^ 16) + 512,
              (unsigned)(B_G + (3 * w + h) * 1024 + (blk * 32 + r16) * 16), (unsigned)(B_G + (3 * w + 2) * 1024 + (h * 32 + r16) * 16 + blk * 8),
              (unsigned)(B_NRM + r16 * 64 + (w * 4 + g4) * 4), WS_FD + (((qt >> 2) * 32 + w * 64 + lane) & 255) * 128};
  unsigned cnt, dl;
  asm volatile(
      "v_mov_b32 v20, 0\n\t"
      "v_mov_b32 v21, 0\n\t"
      "v_mov_b32 v22, 0\n\t"
      "v_mov_b32 v23, 0\n\t"
      "s_waitcnt vmcnt(0)\n\t"
      "s_barrier\n\t"
      "s_cmp_eq_u32 %[w], 0\n\t"
      "s_cbranch_scc0 58f\n\t"
      "s_sleep 18\n\t"
      "buffer_load_dwordx2 v[24:25], v7, %[rs], 0 offen sc1\n\t"
      "58:\n\t"
      "ds_write_b128 v6, v[16:19]\n\t"
      "s_mov_b32 %[cnt], 0x10000\n\t"
      "s_cmp_eq_u32 %[w], 0\n\t"
      "s_cbranch_scc0 62f\n\t"
      "61:\n\t"
      "s_sleep 4\n\t"
      "buffer_load_dwordx2 v[26:27], v7, %[rs], 0 offen sc1\n\t"
      "s_waitcnt vmcnt(1)\n\t"
      "v_cmp_ne_u64 vcc, %[tag], v[24:25]\n\t"
      "s_cbranch_vccz 62f\n\t"
      "s_sleep 4\n\t"
      "buffer_load_dwordx2 v[24:25], v7, %[rs], 0 offen sc1\n\t"
      "s_waitcnt vmcnt(1)\n\t"
      "v_cmp_ne_u64 vcc, %[tag], v[26:27]\n\t"
      "s_cbranch_vccz 62f\n\t"
      "s_sub_u32 %[cnt], %[cnt], 1\n\t"
      "s_cbranch_scc0 61b\n\t"
      "62:\n\t"
      "s_waitcnt lgkmcnt(0)\n\t"
      "s_barrier\n\t"
      "s_sleep 4\n\t"
      "s_mov_b32 %[cnt], 0x10000\n\t"
      "67:\n\t"
      "buffer_load_dwordx2 v[28:29], v15, %[rs], 0 offen sc1\n\t"
      "s_barrier\n\t"
      "ds_read_b128 v[64:67], v10 offset:0\n\t"
      "ds_read_b128 v[68:71], v10 offset:2048\n\t"
      "ds_read_b128 v[72:75], v10 offset:4096\n\t"
      "ds_read_b128 v[76:79], v10 offset:6144\n\t"
      "ds_read_b128 v[80:83], v10 offset:8192\n\t"
      "ds_read_b128 v[84:87], v10 offset:10240\n\t"
      "ds_read_b128 v[88:91], v11 offset:0\n\t"
      "ds_read_b128 v[92:95], v11 offset:2048\n\t"
      "ds_read_b128 v[96:99], v11 offset:4096\n\t"
      "ds_read_b128 v[100:103], v11 offset:6144\n\t"
      "ds_read_b128 v[104:107], v11 offset:8192\n\t"
      "ds_read_b128 v[108:111], v11 offset:10240\n\t"
      "s_waitcnt lgkmcnt(11)\n\t"
      "v_mfma_f32_16x16x32_bf16 v[232:235], v[160:163], v[64:67], 0\n\t"
      "v_mfma_f32_16x16x32_bf16 v[236:239], v[184:187], v[64:67], 0\n\t"
      "s_waitcnt lgkmcnt(10)\n\t"
      "v_mfma_f32_16x16x32_bf16 v[232:235], v[164:167], v[68:71], v[232:235]\n\t"
      "v_mfma_f32_16x16x32_bf16 v[236:239], v[188:191], v[68:71], v[236:239]\n\t"
      "s_waitcnt lgkmcnt(9)\n\t"
      "v_mfma_f32_16x16x32_bf16 v[232:235], v[168:171], v[72:75], v[232:235]\n\t"
      "v_mfma_f32_16x16x32_bf16 v[236:239], v[192:195], v[72:75], v[236:239]\n\t"
      "s_waitcnt lgkmcnt(8)\n\t"
      "v_mfma_f32_16x16x32_bf16 v[232:235], v[172:175], v[76:79], v[232:235]\n\t"
      "v_mfma_f32_16x16x32_bf16 v[236:239], v[196:199], v[76:79], v[236:239]\n\t"
      "s_waitcnt lgkmcnt(7)\n\t"
      "v_mfma_f32_16x16x32_bf16 v[232:235], v[176:179], v[80:83], v[232:235]\n\t"
      "v_mfma_f32_16x16x32_bf16 v[236:239], v[200:203], v[80:83], v[236:239]\n\t"
      "s_waitcnt lgkmcnt(6)\n\t"
      "v_mfma_f32_16x16x32_bf16 v[232:235], v[180:183], v[84:87], v[232:235]\n\t"
      "v_mfma_f32_16x16x32_bf16 v[236:239], v[204:207], v[84:87], v[236:239]\n\t"
      "v_mfma_f32_16x16x32_bf16 v[240:243], v[208:211], v[64:67], 0\n\t"
      "s_nop 0\n\t"
      "v_mfma_f32_16x16x32_bf16 v[240:243], v[212:215], v[68:71], v[240:243]\n\t"
      "s_nop 0\n\t"
      "v_mfma_f32_16x16x32_bf16 v[240:243], v[216:219], v[72:75], v[240:243]\n\t"
      "s_nop 0\n\t"
      "s_waitcnt lgkmcnt(0)\n\t"
      "v_mfma_f32_16x16x32_bf16 v[240:243], v[220:223], v[76:79], v[240:243]\n\t"
      "v_cvt_pk_bf16_f32 v40, v232, v233\n\t"
      "v_cvt_pk_bf16_f32 v41, v234, v235\n\t"
      "v_mfma_f32_16x16x32_bf16 v[240:243], v[224:227], v[80:83], v[240:243]\n\t"
      "v_cvt_pk_bf16_f32 v42, v236, v237\n\t"
      "v_cvt_pk_bf16_f32 v43, v238, v239\n\t"
      "v_mfma_f32_16x16x32_bf16 v[240:243], v[228:231], v[84:87], v[240:243]\n\t"
      "ds_write_b128 v12, v[40:43] offset:0\n\t"
      "v_mul_f32 v56, v232, v232\n\t"
      "v_mfma_f32_16x16x32_bf16 v[244:247], v[160:163], v[88:91], 0\n\t"
      "v_mul_f32 v57, v233, v233\n\t"
      "v_fmac_f32 v56, v234, v234\n\t"
      "v_mfma_f32_16x16x32_bf16 v[248:251], v[184:187], v[88:91], 0\n\t"
      "v_fmac_f32 v57, v235, v235\n\t"
      "v_fmac_f32 v56, v236, v236\n\t"
      "v_mfma_f32_16x16x32_bf16 v[244:247], v[164:167], v[92:95], v[244:247]\n\t"
      "v_fmac_f32 v57, v237, v237\n\t"
      "v_fmac_f32 v56, v238, v238\n\t"
      "v_mfma_f32_16x16x32_bf16 v[248:251], v[188:191], v[92:95], v[248:251]\n\t"
      "v_fmac_f32 v57, v239, v239\n\t"
      "v_cvt_pk_bf16_f32 v44, v240, v241\n\t"
      "v_mfma_f32_16x16x32_bf16 v[244:247], v[168:171], v[96:99], v[244:247]\n\t"
      "v_cvt_pk_bf16_f32 v45, v242, v243\n\t"
      "ds_write_b64 v13, v[44:45] offset:0\n\t"
      "v_mfma_f32_16x16x32_bf16 v[248:251], v[192:195], v[96:99], v[248:251]\n\t"
      "v_fmac_f32 v56, v240, v240\n\t"
      "v_fmac_f32 v57, v241, v241\n\t"
      "v_mfma_f32_16x16x32_bf16 v[244:247], v[172:175], v[100:103], v[244:247]\n\t"
      "v_fmac_f32 v56, v242, v242\n\t"
      "v_fmac_f32 v57, v243, v243\n\t"
      "v_mfma_f32_16x16x32_bf16 v[248:251], v[196:199], v[100:103], v[248:251]\n\t"
      "v_add_f32 v56, v56, v57\n\t"
      "ds_write_b32 v14, v56 offset:0\n\t"
      "v_mfma_f32_16x16x32_bf16 v[244:247], v[176:179], v[104:107], v[244:247]\n\t"
      "v_mfma_f32_16x16x32_bf16 v[248:251], v[200:203], v[104:107], v[248:251]\n\t"
      "v_mfma_f32_16x16x32_bf16 v[244:247], v[180:183], v[108:111], v[244:247]\n\t"
      "v_mfma_f32_16x16x32_bf16 v[248:251], v[204:207], v[108:111], v[248:251]\n\t"
      "v_mfma_f32_16x16x32_bf16 v[252:255], v[208:211], v[88:91], 0\n\t"
      "s_waitcnt vmcnt(0)\n\t"
      "v_cmp_ne_u64 vcc, %[tag], v[28:29]\n\t"
      "v_mfma_f32_16x16x32_bf16 v[252:255], v[212:215], v[92:95], v[252:255]\n\t"
      "s_nop 0\n\t"
      "s_nop 0\n\t"
      "v_mfma_f32_16x16x32_bf16 v[252:255], v[216:219], v[96:99], v[252:255]\n\t"
      "s_nop 0\n\t"
      "v_cvt_pk_bf16_f32 v48, v244, v245\n\t"
      "v_cvt_pk_bf16_f32 v49, v246, v247\n\t"
      "v_mfma_f32_16x16x32_bf16 v[252:255], v[220:223], v[100:103], v[252:255]\n\t"
      "v_cvt_pk_bf16_f32 v50, v248, v249\n\t"
      "v_cvt_pk_bf16_f32 v51, v250, v251\n\t"
      "v_mfma_f32_16x16x32_bf16 v[252:255], v[224:227], v[104:107], v[252:255]\n\t"
      "ds_write_b128 v12, v[48:51] offset:256\n\t"
      "v_mul_f32 v58, v244, v244\n\t"
      "v_mfma_f32_16x16x32_bf16 v[252:255], v[228:231], v[108:111], v[252:255]\n\t"
      "v_mul_f32 v59, v245, v245\n\t"
      "v_fmac_f32 v58, v246, v246\n\t"
      "v_fmac_f32 v59, v247, v247\n\t"
      "v_fmac_f32 v58, v248, v248\n\t"
      "v_fmac_f32 v59, v249, v249\n\t"
      "v_fmac_f32 v58, v250, v250\n\t"
      "v_fmac_f32 v59, v251, v251\n\t"
      "s_nop 0\n\t"
      "v_cvt_pk_bf16_f32 v52, v252, v253\n\t"
      "v_cvt_pk_bf16_f32 v53, v254, v255\n\t"
      "ds_write_b64 v13, v[52:53] offset:256\n\t"
      "v_fmac_f32 v58, v252, v252\n\t"
      "v_fmac_f32 v59, v253, v253\n\t"
      "v_fmac_f32 v58, v254, v254\n\t"
      "v_fmac_f32 v59, v255, v255\n\t"
      "v_add_f32 v58, v58, v59\n\t"
      "ds_write_b32 v14, v58 offset:1024\n\t"
      "s_cbranch_vccnz 63f\n\t"
      "64:\n\t"
      "s_waitcnt lgkmcnt(0)\n\t"
      "s_barrier\n\t"
      "ds_read_b128 v[112:115], v8 offset:0\n\t"
      "ds_read_b128 v[40:43], v1 offset:0\n\t"
      "ds_read_b128 v[116:119], v8 offset:1024\n\t"
      "ds_read_b128 v[44:47], v2 offset:1024\n\t"
      "ds_read_b128 v[120:123], v8 offset:2048\n\t"
      "ds_read_b128 v[48:51], v1 offset:2048\n\t"
      "ds_read_b128 v[124:127], v8 offset:3072\n\t"
      "ds_read_b128 v[52:55], v2 offset:3072\n\t"
      "ds_read_b128 v[128:131], v8 offset:4096\n\t"
      "ds_read_b128 v[56:59], v1 offset:4096\n\t"
      "ds_read_b128 v[132:135], v8 offset:5120\n\t"
      "ds_read_b128 v[60:63], v2 offset:5120\n\t"
      "s_mov_b32 %[cnt], 6\n\t"
      "s_mov_b32 %[dl], %[grp]\n\t"
      "s_waitcnt lgkmcnt(10)\n\t"
      "v_mfma_f32_32x32x16_bf16 v[24:39], v[40:43], v[112:115], 0\n\t"
      "ds_read_b128 v[136:139], v8 offset:6144\n\t"
      "ds_read_b128 v[40:43], v1 offset:6144\n\t"
      "v_lshlrev_b32 v9, 2, v5\n\t"
      "v_lshrrev_b32 v10, 4, v5\n\t"
      "s_waitcnt lgkmcnt(10)\n\t"
      "v_mfma_f32_32x32x16_bf16 v[24:39], v[44:47], v[116:119], v[24:39]\n\t"
      "ds_read_b128 v[140:143], v8 offset:7168\n\t"
      "ds_read_b128 v[44:47], v2 offset:7168\n\t"
      "v_and_b32 v9, 0x7c0, v9\n\t"
      "v_and_b32 v10, 32, v10\n\t"
      "s_waitcnt lgkmcnt(10)\n\t"
      "v_mfma_f32_32x32x16_bf16 v[24:39], v[48:51], v[120:123], v[24:39]\n\t"
      "ds_read_b128 v[144:147], v8 offset:8192\n\t"
      "ds_read_b128 v[48:51], v1 offset:8192\n\t"
      "v_add_u32 v10, 161792, v10\n\t"
      "v_add_u32 v9, v9, v10\n\t"
      "s_waitcnt lgkmcnt(10)\n\t"
      "v_mfma_f32_32x32x16_bf16 v[24:39], v[52:55], v[124:127], v[24:39]\n\t"
      "ds_read_b128 v[148:151], v8 offset:9216\n\t"
      "ds_read_b128 v[52:55], v2 offset:9216\n\t"
      "s_waitcnt lgkmcnt(10)\n\t"
      "v_mfma_f32_32x32x16_bf16 v[24:39], v[56:59], v[128:131], v[24:39]\n\t"
      "ds_read_b128 v[152:155], v8 offset:10240\n\t"
      "ds_read_b128 v[56:59], v1 offset:10240\n\t"
      "s_waitcnt lgkmcnt(10)\n\t"
      "v_mfma_f32_32x32x16_bf16 v[24:39], v[60:63], v[132:135], v[24:39]\n\t"
      "ds_read_b128 v[156:159], v8 offset:11264\n\t"
      "ds_read_b128 v[60:63], v2 offset:11264\n\t"
      "s_waitcnt lgkmcnt(10)\n\t"
      "v_mfma_f32_32x32x16_bf16 v[24:39], v[40:43], v[136:139], v[24:39]\n\t"
      "ds_read_b128 v[64:67], v9\n\t"
      "v_add_u32 v1, %[grp], v1\n\t"
      "v_add_u32 v2, %[grp], v2\n\t"
      "s_waitcnt lgkmcnt(9)\n\t"
      "v_mfma_f32_32x32x16_bf16 v[24:39], v[44:47], v[140:143], v[24:39]\n\t"
      "ds_read_b128 v[68:71], v9 offset:16\n\t"
      "s_waitcnt lgkmcnt(8)\n\t"
      "v_mfma_f32_32x32x16_bf16 v[24:39], v[48:51], v[144:147], v[24:39]\n\t"
      "s_waitcnt lgkmcnt(6)\n\t"
      "v_mfma_f32_32x32x16_bf16 v[24:39], v[52:55], v[148:151], v[24:39]\n\t"
      "s_waitcnt lgkmcnt(4)\n\t"
      "v_mfma_f32_32x32x16_bf16 v[24:39], v[56:59], v[152:155], v[24:39]\n\t"
      "s_waitcnt lgkmcnt(0)\n\t"
      "v_mfma_f32_32x32x16_bf16 v[24:39], v[60:63], v[156:159], v[24:39]\n\t"
      "s_barrier\n\t"
      "v_add_f32 v64, v64, v65\n\t"
      "v_add_f32 v66, v66, v67\n\t"
      "v_add_f32 v68, v68, v69\n\t"
      "v_add_f32 v70, v70, v71\n\t"
      "v_add_f32 v64, v64, v66\n\t"
      "v_add_f32 v68, v68, v70\n\t"
      "v_add_f32 v64, v64, v68\n\t"
      "v_mov_b32 v65, v64\n\t"
      "s_nop 1\n\t"
      "v_permlane32_swap_b32 v64, v65\n\t"
      "v_add_f32 v64, v64, v65\n\t"
      "v_cmp_nge_f32 vcc, %[lim], v64\n\t"
      "s_cbranch_vccnz 30f\n\t"
      "v_mfma_f32_32x32x16_bf16 v[160:175], v[20:23], v[20:23], 0\n\t"
      "ds_read_b128 v[40:43], v1 offset:0\n\t"
      "ds_read_b128 v[44:47], v2 offset:1024\n\t"
      "v_mfma_f32_32x32x16_bf16 v[176:191], v[20:23], v[20:23], 0\n\t"
      "ds_read_b128 v[48:51], v1 offset:2048\n\t"
      "ds_read_b128 v[52:55], v2 offset:3072\n\t"
      "v_exp_f32 v24, v24\n\t"
      "v_mfma_f32_32x32x16_bf16 v[192:207], v[20:23], v[20:23], 0\n\t"
      "v_exp_f32 v25, v25\n\t"
      "v_exp_f32 v26, v26\n\t"
      "ds_read_b128 v[56:59], v1 offset:4096\n\t"
      "v_mfma_f32_32x32x16_bf16 v[208:223], v[20:23], v[20:23], 0\n\t"
      "v_exp_f32 v27, v27\n\t"
      "v_exp_f32 v28, v28\n\t"
      "ds_read_b128 v[60:63], v2 offset:5120\n\t"
      "v_mfma_f32_32x32x16_bf16 v[224:239], v[20:23], v[20:23], 0\n\t"
      "v_exp_f32 v29, v29\n\t"
      "v_exp_f32 v30, v30\n\t"
      "v_add_f32 v0, v0, v24\n\t"
      "v_mfma_f32_32x32x16_bf16 v[240:255], v[20:23], v[20:23], 0\n\t"
      "v_exp_f32 v31, v31\n\t"
      "v_exp_f32 v8, v32\n\t"
      "v_add_f32 v0, v0, v25\n\t"
      "v_exp_f32 v9, v33\n\t"
      "v_exp_f32 v10, v34\n\t"
      "v_cvt_pk_bf16_f32 v16, v24, v25\n\t"
      "v_exp_f32 v11, v35\n\t"
      "v_exp_f32 v12, v36\n\t"
      "v_cvt_pk_bf16_f32 v17, v26, v27\n\t"
      "v_exp_f32 v13, v37\n\t"
      "v_exp_f32 v14, v38\n\t"
      "v_cvt_pk_bf16_f32 v18, v28, v29\n\t"
      "v_exp_f32 v15, v39\n\t"
      "v_cvt_pk_bf16_f32 v19, v30, v31\n\t"
      "v_add_f32 v0, v0, v26\n\t"
      "v_add_f32 v0, v0, v27\n\t"
      "v_add_f32 v0, v0, v28\n\t"
      "v_add_f32 v0, v0, v29\n\t"
      "v_add_f32 v0, v0, v30\n\t"
      "v_add_f32 v0, v0, v31\n\t"
      "s_waitcnt lgkmcnt(0)\n\t"
      "s_branch 4f\n\t"
      "1:\n\t"
      "v_mfma_f32_32x32x16_bf16 v[240:255], v[108:111], v[20:23], v[240:255]\n\t"
      "4:\n\t"
      "s_barrier\n\t"
      "v_mfma_f32_32x32x16_bf16 v[24:39], v[40:43], v[112:115], 0\n\t"
      "ds_read_b128 v[40:43], v1 offset:6144\n\t"
      "ds_read_b64_tr_b16 v[64:65], v3 offset:0\n\t"
      "ds_read_b64_tr_b16 v[66:67], v3 offset:256\n\t"
      "v_cvt_pk_bf16_f32 v20, v8, v9\n\t"
      "v_cvt_pk_bf16_f32 v21, v10, v11\n\t"
      "v_mfma_f32_32x32x16_bf16 v[24:39], v[44:47], v[116:119], v[24:39]\n\t"
      "ds_read_b128 v[44:47], v2 offset:7168\n\t"
      "ds_read_b64_tr_b16 v[72:73], v3 offset:2048\n\t"
      "ds_read_b64_tr_b16 v[74:75], v3 offset:2304\n\t"
      "v_cvt_pk_bf16_f32 v22, v12, v13\n\t"
      "v_cvt_pk_bf16_f32 v23, v14, v15\n\t"
      "v_mfma_f32_32x32x16_bf16 v[24:39], v[48:51], v[120:123], v[24:39]\n\t"
      "ds_read_b128 v[48:51], v1 offset:8192\n\t"
      "ds_read_b64_tr_b16 v[80:81], v3 offset:4096\n\t"
      "ds_read_b64_tr_b16 v[82:83], v3 offset:4352\n\t"
      "v_add_f32 v0, v0, v8\n\t"
      "v_add_f32 v0, v0, v9\n\t"
      "v_mfma_f32_32x32x16_bf16 v[24:39], v[52:55], v[124:127], v[24:39]\n\t"
      "ds_read_b128 v[52:55], v2 offset:9216\n\t"
      "ds_read_b64_tr_b16 v[88:89], v3 offset:6144\n\t"
      "ds_read_b64_tr_b16 v[90:91], v3 offset:6400\n\t"
      "v_add_f32 v0, v0, v10\n\t"
      "v_add_f32 v0, v0, v11\n\t"
      "v_mfma_f32_32x32x16_bf16 v[24:39], v[56:59], v[128:131], v[24:39]\n\t"
      "ds_read_b128 v[56:59], v1 offset:10240\n\t"
      "ds_read_b64_tr_b16 v[96:97], v3 offset:8192\n\t"
      "ds_read_b64_tr_b16 v[98:99], v3 offset:8448\n\t"
      "v_add_f32 v0, v0, v12\n\t"
      "v_add_f32 v0, v0, v13\n\t"
      "v_mfma_f32_32x32x16_bf16 v[24:39], v[60:63], v[132:135], v[24:39]\n\t"
      "ds_read_b128 v[60:63], v2 offset:11264\n\t"
      "ds_read_b64_tr_b16 v[104:105], v3 offset:10240\n\t"
      "ds_read_b64_tr_b16 v[106:107], v3 offset:10496\n\t"
      "v_add_f32 v0, v0, v14\n\t"
      "s_waitcnt lgkmcnt(14)\n\t"
      "v_mfma_f32_32x32x16_bf16 v[24:39], v[40:43], v[136:139], v[24:39]\n\t"
      "ds_read_b64_tr_b16 v[68:69], v4 offset:0\n\t"
      "ds_read_b64_tr_b16 v[70:71], v4 offset:256\n\t"
      "ds_read_b64_tr_b16 v[76:77], v4 offset:2048\n\t"
      "ds_read_b64_tr_b16 v[78:79], v4 offset:2304\n\t"
      "v_add_u32 v3, %[dl], v3\n\t"
      "v_mfma_f32_32x32x16_bf16 v[24:39], v[44:47], v[140:143], v[24:39]\n\t"
      "ds_read_b64_tr_b16 v[84:85], v4 offset:4096\n\t"
      "ds_read_b64_tr_b16 v[86:87], v4 offset:4352\n\t"
      "ds_read_b64_tr_b16 v[92:93], v4 offset:6144\n\t"
      "ds_read_b64_tr_b16 v[94:95], v4 offset:6400\n\t"
      "s_waitcnt lgkmcnt(15)\n\t"
      "v_mfma_f32_32x32x16_bf16 v[24:39], v[48:51], v[144:147], v[24:39]\n\t"
      "ds_read_b64_tr_b16 v[100:101], v4 offset:8192\n\t"
      "ds_read_b64_tr_b16 v[102:103], v4 offset:8448\n\t"
      "ds_read_b64_tr_b16 v[108:109], v4 offset:10240\n\t"
      "ds_read_b64_tr_b16 v[110:111], v4 offset:10496\n\t"
      "v_mfma_f32_32x32x16_bf16 v[24:39], v[52:55], v[148:151], v[24:39]\n\t"
      "v_add_f32 v0, v0, v15\n\t"
      "v_add_u32 v4, %[dl], v4\n\t"
      "s_bitcmp1_b32 %[msk], %[cnt]\n\t"
      "s_waitcnt lgkmcnt(14)\n\t"
      "v_mfma_f32_32x32x16_bf16 v[24:39], v[56:59], v[152:155], v[24:39]\n\t"
      "s_cselect_b32 %[dl], 0xfffe8000, %[grp]\n\t"
      "v_add_u32 v1, %[dl], v1\n\t"
      "v_add_u32 v2, %[dl], v2\n\t"
      "s_sub_u32 %[cnt], %[cnt], 1\n\t"
      "v_mfma_f32_32x32x16_bf16 v[24:39], v[60:63], v[156:159], v[24:39]\n\t"
      "s_waitcnt lgkmcnt(0)\n\t"
      "s_barrier\n\t"
      "s_cbranch_scc1 5f\n\t"
      "v_mfma_f32_32x32x16_bf16 v[160:175], v[64:67], v[16:19], v[160:175]\n\t"
      "ds_read_b128 v[40:43], v1 offset:0\n\t"
      "ds_read_b128 v[44:47], v2 offset:1024\n\t"
      "s_nop 1\n\t"
      "v_mfma_f32_32x32x16_bf16 v[176:191], v[72:75], v[16:19], v[176:191]\n\t"
      "ds_read_b128 v[48:51], v1 offset:2048\n\t"
      "ds_read_b128 v[52:55], v2 offset:3072\n\t"
      "s_nop 0\n\t"
      "v_exp_f32 v24, v24\n\t"
      "v_mfma_f32_32x32x16_bf16 v[192:207], v[80:83], v[16:19], v[192:207]\n\t"
      "v_exp_f32 v25, v25\n\t"
      "v_exp_f32 v26, v26\n\t"
      "ds_read_b128 v[56:59], v1 offset:4096\n\t"
      "v_mfma_f32_32x32x16_bf16 v[208:223], v[88:91], v[16:19], v[208:223]\n\t"
      "v_exp_f32 v27, v27\n\t"
      "v_exp_f32 v28, v28\n\t"
      "ds_read_b128 v[60:63], v2 offset:5120\n\t"
      "v_mfma_f32_32x32x16_bf16 v[224:239], v[96:99], v[16:19], v[224:239]\n\t"
      "v_exp_f32 v29, v29\n\t"
      "v_exp_f32 v30, v30\n\t"
      "v_add_f32 v0, v0, v24\n\t"
      "v_mfma_f32_32x32x16_bf16 v[240:255], v[104:107], v[16:19], v[240:255]\n\t"
      "v_exp_f32 v31, v31\n\t"
      "v_exp_f32 v8, v32\n\t"
      "v_add_f32 v0, v0, v25\n\t"
      "v_mfma_f32_32x32x16_bf16 v[160:175], v[68:71], v[20:23], v[160:175]\n\t"
      "v_exp_f32 v9, v33\n\t"
      "v_exp_f32 v10, v34\n\t"
      "v_cvt_pk_bf16_f32 v16, v24, v25\n\t"
      "v_mfma_f32_32x32x16_bf16 v[176:191], v[76:79], v[20:23], v[176:191]\n\t"
      "v_exp_f32 v11, v35\n\t"
      "v_exp_f32 v12, v36\n\t"
      "v_cvt_pk_bf16_f32 v17, v26, v27\n\t"
      "v_mfma_f32_32x32x16_bf16 v[192:207], v[84:87], v[20:23], v[192:207]\n\t"
      "v_exp_f32 v13, v37\n\t"
      "v_exp_f32 v14, v38\n\t"
      "v_cvt_pk_bf16_f32 v18, v28, v29\n\t"
      "v_mfma_f32_32x32x16_bf16 v[208:223], v[92:95], v[20:23], v[208:223]\n\t"
      "v_exp_f32 v15, v39\n\t"
      "v_cvt_pk_bf16_f32 v19, v30, v31\n\t"
      "v_add_f32 v0, v0, v26\n\t"
      "v_add_f32 v0, v0, v27\n\t"
      "v_mfma_f32_32x32x16_bf16 v[224:239], v[100:103], v[20:23], v[224:239]\n\t"
      "v_add_f32 v0, v0, v28\n\t"
      "v_add_f32 v0, v0, v29\n\t"
      "v_add_f32 v0, v0, v30\n\t"
      "v_add_f32 v0, v0, v31\n\t"
      "s_waitcnt lgkmcnt(0)\n\t"
      "s_branch 1b\n\t"
      "5:\n\t"
      "v_mfma_f32_32x32x16_bf16 v[160:175], v[64:67], v[16:19], v[160:175]\n\t"
      "ds_read_b64_tr_b16 v[64:65], v3 offset:0\n\t"
      "ds_read_b64_tr_b16 v[66:67], v3 offset:256\n\t"
      "v_add_u32 v1, 98304, v5\n\t"
      "v_add_u32 v2, 147456, v5\n\t"
      "v_and_b32 v7, 0x1f0, v5\n\t"
      "v_mfma_f32_32x32x16_bf16 v[176:191], v[72:75], v[16:19], v[176:191]\n\t"
      "ds_read_b64_tr_b16 v[72:73], v3 offset:2048\n\t"
      "ds_read_b64_tr_b16 v[74:75], v3 offset:2304\n\t"
      "v_lshrrev_b32 v40, 7, v5\n\t"
      "v_and_b32 v40, 4, v40\n\t"
      "v_lshl_add_u32 v7, v7, 1, v40\n\t"
      "v_mfma_f32_32x32x16_bf16 v[192:207], v[80:83], v[16:19], v[192:207]\n\t"
      "ds_read_b64_tr_b16 v[80:81], v3 offset:4096\n\t"
      "ds_read_b64_tr_b16 v[82:83], v3 offset:4352\n\t"
      "v_exp_f32 v24, v24\n\t"
      "v_exp_f32 v25, v25\n\t"
      "v_mfma_f32_32x32x16_bf16 v[208:223], v[88:91], v[16:19], v[208:223]\n\t"
      "ds_read_b64_tr_b16 v[88:89], v3 offset:6144\n\t"
      "ds_read_b64_tr_b16 v[90:91], v3 offset:6400\n\t"
      "v_exp_f32 v26, v26\n\t"
      "v_exp_f32 v27, v27\n\t"
      "v_mfma_f32_32x32x16_bf16 v[224:239], v[96:99], v[16:19], v[224:239]\n\t"
      "ds_read_b64_tr_b16 v[96:97], v3 offset:8192\n\t"
      "ds_read_b64_tr_b16 v[98:99], v3 offset:8448\n\t"
      "v_exp_f32 v28, v28\n\t"
      "v_exp_f32 v29, v29\n\t"
      "v_mfma_f32_32x32x16_bf16 v[240:255], v[104:107], v[16:19], v[240:255]\n\t"
      "ds_read_b64_tr_b16 v[104:105], v3 offset:10240\n\t"
      "ds_read_b64_tr_b16 v[106:107], v3 offset:10496\n\t"
      "v_exp_f32 v30, v30\n\t"
      "v_exp_f32 v31, v31\n\t"
      "v_mfma_f32_32x32x16_bf16 v[160:175], v[68:71], v[20:23], v[160:175]\n\t"
      "ds_read_b64_tr_b16 v[68:69], v4 offset:0\n\t"
      "ds_read_b64_tr_b16 v[70:71], v4 offset:256\n\t"
      "v_exp_f32 v8, v32\n\t"
      "v_exp_f32 v9, v33\n\t"
      "v_cvt_pk_bf16_f32 v16, v24, v25\n\t"
      "v_mfma_f32_32x32x16_bf16 v[176:191], v[76:79], v[20:23], v[176:191]\n\t"
      "ds_read_b64_tr_b16 v[76:77], v4 offset:2048\n\t"
      "ds_read_b64_tr_b16 v[78:79], v4 offset:2304\n\t"
      "v_exp_f32 v10, v34\n\t"
      "v_exp_f32 v11, v35\n\t"
      "v_cvt_pk_bf16_f32 v17, v26, v27\n\t"
      "v_mfma_f32_32x32x16_bf16 v[192:207], v[84:87], v[20:23], v[192:207]\n\t"
      "ds_read_b64_tr_b16 v[84:85], v4 offset:4096\n\t"
      "ds_read_b64_tr_b16 v[86:87], v4 offset:4352\n\t"
      "v_exp_f32 v12, v36\n\t"
      "v_exp_f32 v13, v37\n\t"
      "v_cvt_pk_bf16_f32 v18, v28, v29\n\t"
      "v_mfma_f32_32x32x16_bf16 v[208:223], v[92:95], v[20:23], v[208:223]\n\t"
      "ds_read_b64_tr_b16 v[92:93], v4 offset:6144\n\t"
      "ds_read_b64_tr_b16 v[94:95], v4 offset:6400\n\t"
      "v_exp_f32 v14, v38\n\t"
      "v_exp_f32 v15, v39\n\t"
      "v_cvt_pk_bf16_f32 v19, v30, v31\n\t"
      "v_mfma_f32_32x32x16_bf16 v[224:239], v[100:103], v[20:23], v[224:239]\n\t"
      "ds_read_b64_tr_b16 v[100:101], v4 offset:8192\n\t"
      "ds_read_b64_tr_b16 v[102:103], v4 offset:8448\n\t"
      "v_add_u32 v7, 162816, v7\n\t"
      "v_add_f32 v0, v0, v24\n\t"
      "v_add_f32 v0, v0, v25\n\t"
      "v_mfma_f32_32x32x16_bf16 v[240:255], v[108:111], v[20:23], v[240:255]\n\t"
      "ds_read_b64_tr_b16 v[108:109], v4 offset:10240\n\t"
      "ds_read_b64_tr_b16 v[110:111], v4 offset:10496\n\t"
      "v_cvt_pk_bf16_f32 v20, v8, v9\n\t"
      "v_cvt_pk_bf16_f32 v21, v10, v11\n\t"
      "v_cvt_pk_bf16_f32 v22, v12, v13\n\t"
      "v_cvt_pk_bf16_f32 v23, v14, v15\n\t"
      "ds_read_b128 v[40:43], v2 offset:12288\n\t"
      "ds_read_b128 v[44:47], v2 offset:13312\n\t"
      "31:\n\t"
      "s_cmp_eq_u32 %[w], 0\n\t"
      "s_cbranch_scc1 20f\n\t"
      "s_cmp_eq_u32 %[w], 1\n\t"
      "s_cbranch_scc1 21f\n\t"
      "s_cmp_eq_u32 %[w], 2\n\t"
      "s_cbranch_scc1 22f\n\t"
      "s_waitcnt lgkmcnt(14)\n\t"
      "v_mfma_f32_32x32x16_bf16 v[160:175], v[64:67], v[16:19], v[160:175]\n\t"
      "v_add_f32 v0, v0, v26\n\t"
      "v_add_f32 v0, v0, v27\n\t"
      "v_add_f32 v0, v0, v28\n\t"
      "v_add_f32 v0, v0, v29\n\t"
      "v_add_f32 v0, v0, v30\n\t"
      "s_waitcnt lgkmcnt(12)\n\t"
      "v_mfma_f32_32x32x16_bf16 v[160:175], v[68:71], v[20:23], v[160:175]\n\t"
      "v_add_f32 v0, v0, v31\n\t"
      "v_add_f32 v0, v0, v8\n\t"
      "v_add_f32 v0, v0, v9\n\t"
      "v_add_f32 v0, v0, v10\n\t"
      "v_add_f32 v0, v0, v11\n\t"
      "v_mfma_f32_32x32x16_bf16 v[176:191], v[72:75], v[16:19], v[176:191]\n\t"
      "v_add_f32 v0, v0, v12\n\t"
      "v_add_f32 v0, v0, v13\n\t"
      "v_add_f32 v0, v0, v14\n\t"
      "v_add_f32 v0, v0, v15\n\t"
      "ds_write_b32 v7, v0 offset:24\n\t"
      "s_waitcnt lgkmcnt(1)\n\t"
      "v_mfma_f32_32x32x16_bf16 v[176:191], v[76:79], v[20:23], v[176:191]\n\t"
      "s_nop 4\n\t"
      "v_mfma_f32_32x32x16_bf16 v[192:207], v[80:83], v[16:19], v[192:207]\n\t"
      "v_cvt_pk_bf16_f32 v112, v160, v161\n\t"
      "v_cvt_pk_bf16_f32 v113, v162, v163\n\t"
      "v_cvt_pk_bf16_f32 v114, v164, v165\n\t"
      "v_cvt_pk_bf16_f32 v115, v166, v167\n\t"
      "ds_write_b128 v1, v[112:115] offset:36864\n\t"
      "v_mfma_f32_32x32x16_bf16 v[192:207], v[84:87], v[20:23], v[192:207]\n\t"
      "v_cvt_pk_bf16_f32 v116, v168, v169\n\t"
      "v_cvt_pk_bf16_f32 v117, v170, v171\n\t"
      "v_cvt_pk_bf16_f32 v118, v172, v173\n\t"
      "v_cvt_pk_bf16_f32 v119, v174, v175\n\t"
      "ds_write_b128 v1, v[116:119] offset:37888\n\t"
      "v_mfma_f32_32x32x16_bf16 v[208:223], v[88:91], v[16:19], v[208:223]\n\t"
      "v_cvt_pk_bf16_f32 v120, v176, v177\n\t"
      "v_cvt_pk_bf16_f32 v121, v178, v179\n\t"
      "v_cvt_pk_bf16_f32 v122, v180, v181\n\t"
      "v_cvt_pk_bf16_f32 v123, v182, v183\n\t"
      "ds_write_b128 v1, v[120:123] offset:38912\n\t"
      "v_mfma_f32_32x32x16_bf16 v[208:223], v[92:95], v[20:23], v[208:223]\n\t"
      "v_cvt_pk_bf16_f32 v124, v184, v185\n\t"
      "v_cvt_pk_bf16_f32 v125, v186, v187\n\t"
      "v_cvt_pk_bf16_f32 v126, v188, v189\n\t"
      "v_cvt_pk_bf16_f32 v127, v190, v191\n\t"
      "ds_write_b128 v1, v[124:127] offset:39936\n\t"
      "v_mfma_f32_32x32x16_bf16 v[224:239], v[96:99], v[16:19], v[224:239]\n\t"
      "v_cvt_pk_bf16_f32 v128, v192, v193\n\t"
      "v_cvt_pk_bf16_f32 v129, v194, v195\n\t"
      "v_cvt_pk_bf16_f32 v130, v196, v197\n\t"
      "v_cvt_pk_bf16_f32 v131, v198, v199\n\t"
      "ds_write_b128 v1, v[128:131] offset:40960\n\t"
      "v_mfma_f32_32x32x16_bf16 v[224:239], v[100:103], v[20:23], v[224:239]\n\t"
      "v_cvt_pk_bf16_f32 v132, v200, v201\n\t"
      "v_cvt_pk_bf16_f32 v133, v202, v203\n\t"
      "v_cvt_pk_bf16_f32 v134, v204, v205\n\t"
      "v_cvt_pk_bf16_f32 v135, v206, v207\n\t"
      "ds_write_b128 v1, v[132:135] offset:41984\n\t"
      "v_mfma_f32_32x32x16_bf16 v[240:255], v[104:107], v[16:19], v[240:255]\n\t"
      "v_cvt_pk_bf16_f32 v136, v208, v209\n\t"
      "v_cvt_pk_bf16_f32 v137, v210, v211\n\t"
      "v_cvt_pk_bf16_f32 v138, v212, v213\n\t"
      "v_cvt_pk_bf16_f32 v139, v214, v215\n\t"
      "ds_write_b128 v1, v[136:139] offset:43008\n\t"
      "v_mfma_f32_32x32x16_bf16 v[240:255], v[108:111], v[20:23], v[240:255]\n\t"
      "v_cvt_pk_bf16_f32 v140, v216, v217\n\t"
      "v_cvt_pk_bf16_f32 v141, v218, v219\n\t"
      "v_cvt_pk_bf16_f32 v142, v220, v221\n\t"
      "v_cvt_pk_bf16_f32 v143, v222, v223\n\t"
      "ds_write_b128 v1, v[140:143] offset:44032\n\t"
      "v_cvt_pk_bf16_f32 v144, v224, v225\n\t"
      "v_cvt_pk_bf16_f32 v145, v226, v227\n\t"
      "v_cvt_pk_bf16_f32 v146, v228, v229\n\t"
      "v_cvt_pk_bf16_f32 v147, v230, v231\n\t"
      "ds_write_b128 v1, v[144:147] offset:45056\n\t"
      "s_waitcnt vmcnt(0) lgkmcnt(0)\n\t"
      "s_barrier\n\t"
      "ds_read_b128 v[64:67], v1 offset:9216\n\t"
      "ds_read_b128 v[68:71], v1 offset:21504\n\t"
      "ds_read_b128 v[72:75], v1 offset:33792\n\t"
      "v_and_b32 v7, 0xffffffe0, v7\n\t"
      "ds_read_b128 v[48:51], v7\n\t"
      "ds_read_b128 v[52:55], v7 offset:16\n\t"
      "ds_read_b128 v[76:79], v1 offset:10240\n\t"
      "ds_read_b128 v[80:83], v1 offset:22528\n\t"
      "ds_read_b128 v[84:87], v1 offset:34816\n\t"
      "ds_read_b128 v[88:91], v1 offset:11264\n\t"
      "ds_read_b128 v[92:95], v1 offset:23552\n\t"
      "ds_read_b128 v[96:99], v1 offset:35840\n\t"
      "s_waitcnt lgkmcnt(10)\n\t"
      "v_mfma_f32_32x32x16_bf16 v[224:239], v[44:47], v[64:67], v[224:239]\n\t"
      "s_waitcnt lgkmcnt(9)\n\t"
      "v_mfma_f32_32x32x16_bf16 v[224:239], v[44:47], v[68:71], v[224:239]\n\t"
      "s_waitcnt lgkmcnt(8)\n\t"
      "v_mfma_f32_32x32x16_bf16 v[224:239], v[44:47], v[72:75], v[224:239]\n\t"
      "s_waitcnt lgkmcnt(5)\n\t"
      "v_mfma_f32_32x32x16_bf16 v[240:255], v[40:43], v[76:79], v[240:255]\n\t"
      "v_add_f32 v48, v48, v49\n\t"
      "v_add_f32 v50, v50, v51\n\t"
      "v_add_f32 v52, v52, v53\n\t"
      "v_add_f32 v54, v54, v55\n\t"
      "s_waitcnt lgkmcnt(4)\n\t"
      "v_mfma_f32_32x32x16_bf16 v[240:255], v[40:43], v[80:83], v[240:255]\n\t"
      "v_add_f32 v48, v48, v50\n\t"
      "v_add_f32 v52, v52, v54\n\t"
      "v_add_f32 v48, v48, v52\n\t"
      "v_rcp_f32 v48, v48\n\t"
      "s_waitcnt lgkmcnt(3)\n\t"
      "v_mfma_f32_32x32x16_bf16 v[240:255], v[40:43], v[84:87], v[240:255]\n\t"
      "s_nop 0\n\t"
      "v_mul_f32 v232, v48, v232\n\t"
      "v_mul_f32 v233, v48, v233\n\t"
      "v_mul_f32 v234, v48, v234\n\t"
      "v_mul_f32 v235, v48, v235\n\t"
      "s_waitcnt lgkmcnt(2)\n\t"
      "v_mfma_f32_32x32x16_bf16 v[240:255], v[44:47], v[88:91], v[240:255]\n\t"
      "v_mul_f32 v236, v48, v236\n\t"
      "v_mul_f32 v237, v48, v237\n\t"
      "v_mul_f32 v238, v48, v238\n\t"
      "v_mul_f32 v239, v48, v239\n\t"
      "v_cvt_pk_bf16_f32 v148, v232, v233\n\t"
      "s_waitcnt lgkmcnt(1)\n\t"
      "v_mfma_f32_32x32x16_bf16 v[240:255], v[44:47], v[92:95], v[240:255]\n\t"
      "v_cvt_pk_bf16_f32 v149, v234, v235\n\t"
      "v_cvt_pk_bf16_f32 v150, v236, v237\n\t"
      "v_cvt_pk_bf16_f32 v151, v238, v239\n\t"
      "s_waitcnt lgkmcnt(0)\n\t"
      "v_mfma_f32_32x32x16_bf16 v[240:255], v[44:47], v[96:99], v[240:255]\n\t"
      "ds_write_b128 v2, v[148:151] offset:9216\n\t"
      "s_nop 11\n\t"
      "v_mul_f32 v240, v48, v240\n\t"
      "v_mul_f32 v241, v48, v241\n\t"
      "v_mul_f32 v242, v48, v242\n\t"
      "v_mul_f32 v243, v48, v243\n\t"
      "v_mul_f32 v244, v48, v244\n\t"
      "v_mul_f32 v245, v48, v245\n\t"
      "v_mul_f32 v246, v48, v246\n\t"
      "v_mul_f32 v247, v48, v247\n\t"
      "v_cvt_pk_bf16_f32 v152, v240, v241\n\t"
      "v_cvt_pk_bf16_f32 v153, v242, v243\n\t"
      "v_cvt_pk_bf16_f32 v154, v244, v245\n\t"
      "v_cvt_pk_bf16_f32 v155, v246, v247\n\t"
      "ds_write_b128 v2, v[152:155] offset:10240\n\t"
      "v_mul_f32 v248, v48, v248\n\t"
      "v_mul_f32 v249, v48, v249\n\t"
      "v_mul_f32 v250, v48, v250\n\t"
      "v_mul_f32 v251, v48, v251\n\t"
      "v_mul_f32 v252, v48, v252\n\t"
      "v_mul_f32 v253, v48, v253\n\t"
      "v_mul_f32 v254, v48, v254\n\t"
      "v_mul_f32 v255, v48, v255\n\t"
      "v_cvt_pk_bf16_f32 v156, v248, v249\n\t"
      "v_cvt_pk_bf16_f32 v157, v250, v251\n\t"
      "v_cvt_pk_bf16_f32 v158, v252, v253\n\t"
      "v_cvt_pk_bf16_f32 v159, v254, v255\n\t"
      "ds_write_b128 v2, v[156:159] offset:11264\n\t"
      "s_waitcnt lgkmcnt(0)\n\t"
      "s_barrier\n\t"
      "s_branch 29f\n\t"
      "20:\n\t"
      "s_waitcnt lgkmcnt(14)\n\t"
      "v_mfma_f32_32x32x16_bf16 v[192:207], v[80:83], v[16:19], v[192:207]\n\t"
      "v_add_f32 v0, v0, v26\n\t"
      "v_add_f32 v0, v0, v27\n\t"
      "v_add_f32 v0, v0, v28\n\t"
      "v_add_f32 v0, v0, v29\n\t"
      "v_add_f32 v0, v0, v30\n\t"
      "s_waitcnt lgkmcnt(8)\n\t"
      "v_mfma_f32_32x32x16_bf16 v[192:207], v[84:87], v[20:23], v[192:207]\n\t"
      "v_add_f32 v0, v0, v31\n\t"
      "v_add_f32 v0, v0, v8\n\t"
      "v_add_f32 v0, v0, v9\n\t"
      "v_add_f32 v0, v0, v10\n\t"
      "v_add_f32 v0, v0, v11\n\t"
      "v_mfma_f32_32x32x16_bf16 v[208:223], v[88:91], v[16:19], v[208:223]\n\t"
      "v_add_f32 v0, v0, v12\n\t"
      "v_add_f32 v0, v0, v13\n\t"
      "v_add_f32 v0, v0, v14\n\t"
      "v_add_f32 v0, v0, v15\n\t"
      "ds_write_b32 v7, v0 offset:0\n\t"
      "s_waitcnt lgkmcnt(1)\n\t"
      "v_mfma_f32_32x32x16_bf16 v[208:223], v[92:95], v[20:23], v[208:223]\n\t"
      "s_nop 4\n\t"
      "v_mfma_f32_32x32x16_bf16 v[224:239], v[96:99], v[16:19], v[224:239]\n\t"
      "v_cvt_pk_bf16_f32 v128, v192, v193\n\t"
      "v_cvt_pk_bf16_f32 v129, v194, v195\n\t"
      "v_cvt_pk_bf16_f32 v130, v196, v197\n\t"
      "v_cvt_pk_bf16_f32 v131, v198, v199\n\t"
      "ds_write_b128 v1, v[128:131] offset:4096\n\t"
      "v_mfma_f32_32x32x16_bf16 v[224:239], v[100:103], v[20:23], v[224:239]\n\t"
      "v_cvt_pk_bf16_f32 v132, v200, v201\n\t"
      "v_cvt_pk_bf16_f32 v133, v202, v203\n\t"
      "v_cvt_pk_bf16_f32 v134, v204, v205\n\t"
      "v_cvt_pk_bf16_f32 v135, v206, v207\n\t"
      "ds_write_b128 v1, v[132:135] offset:5120\n\t"
      "v_mfma_f32_32x32x16_bf16 v[240:255], v[104:107], v[16:19], v[240:255]\n\t"
      "v_cvt_pk_bf16_f32 v136, v208, v209\n\t"
      "v_cvt_pk_bf16_f32 v137, v210, v211\n\t"
      "v_cvt_pk_bf16_f32 v138, v212, v213\n\t"
      "v_cvt_pk_bf16_f32 v139, v214, v215\n\t"
      "ds_write_b128 v1, v[136:139] offset:6144\n\t"
      "v_mfma_f32_32x32x16_bf16 v[240:255], v[108:111], v[20:23], v[240:255]\n\t"
      "v_cvt_pk_bf16_f32 v140, v216, v217\n\t"
      "v_cvt_pk_bf16_f32 v141, v218, v219\n\t"
      "v_cvt_pk_bf16_f32 v142, v220, v221\n\t"
      "v_cvt_pk_bf16_f32 v143, v222, v223\n\t"
      "ds_write_b128 v1, v[140:143] offset:7168\n\t"
      "v_mfma_f32_32x32x16_bf16 v[176:191], v[72:75], v[16:19], v[176:191]\n\t"
      "v_cvt_pk_bf16_f32 v144, v224, v225\n\t"
      "v_cvt_pk_bf16_f32 v145, v226, v227\n\t"
      "v_cvt_pk_bf16_f32 v146, v228, v229\n\t"
      "v_cvt_pk_bf16_f32 v147, v230, v231\n\t"
      "ds_write_b128 v1, v[144:147] offset:8192\n\t"
      "v_mfma_f32_32x32x16_bf16 v[176:191], v[76:79], v[20:23], v[176:191]\n\t"
      "v_cvt_pk_bf16_f32 v148, v232, v233\n\t"
      "v_cvt_pk_bf16_f32 v149, v234, v235\n\t"
      "v_cvt_pk_bf16_f32 v150, v236, v237\n\t"
      "v_cvt_pk_bf16_f32 v151, v238, v239\n\t"
      "ds_write_b128 v1, v[148:151] offset:9216\n\t"
      "v_mfma_f32_32x32x16_bf16 v[160:175], v[64:67], v[16:19], v[160:175]\n\t"
      "v_cvt_pk_bf16_f32 v152, v240, v241\n\t"
      "v_cvt_pk_bf16_f32 v153, v242, v243\n\t"
      "v_cvt_pk_bf16_f32 v154, v244, v245\n\t"
      "v_cvt_pk_bf16_f32 v155, v246, v247\n\t"
      "ds_write_b128 v1, v[152:155] offset:10240\n\t"
      "v_mfma_f32_32x32x16_bf16 v[160:175], v[68:71], v[20:23], v[160:175]\n\t"
      "v_cvt_pk_bf16_f32 v156, v248, v249\n\t"
      "v_cvt_pk_bf16_f32 v157, v250, v251\n\t"
      "v_cvt_pk_bf16_f32 v158, v252, v253\n\t"
      "v_cvt_pk_bf16_f32 v159, v254, v255\n\t"
      "ds_write_b128 v1, v[156:159] offset:11264\n\t"
      "v_cvt_pk_bf16_f32 v124, v184, v185\n\t"
      "v_cvt_pk_bf16_f32 v125, v186, v187\n\t"
      "v_cvt_pk_bf16_f32 v126, v188, v189\n\t"
      "v_cvt_pk_bf16_f32 v127, v190, v191\n\t"
      "ds_write_b128 v1, v[124:127] offset:3072\n\t"
      "s_waitcnt vmcnt(0) lgkmcnt(0)\n\t"
      "s_barrier\n\t"
      "ds_read_b128 v[64:67], v1 offset:14336\n\t"
      "ds_read_b128 v[68:71], v1 offset:26624\n\t"
      "ds_read_b128 v[72:75], v1 offset:38912\n\t"
      "v_and_b32 v7, 0xffffffe0, v7\n\t"
      "ds_read_b128 v[48:51], v7\n\t"
      "ds_read_b128 v[52:55], v7 offset:16\n\t"
      "ds_read_b128 v[76:79], v1 offset:12288\n\t"
      "ds_read_b128 v[80:83], v1 offset:24576\n\t"
      "ds_read_b128 v[84:87], v1 offset:36864\n\t"
      "ds_read_b128 v[88:91], v1 offset:13312\n\t"
      "ds_read_b128 v[92:95], v1 offset:25600\n\t"
      "ds_read_b128 v[96:99], v1 offset:37888\n\t"
      "s_waitcnt lgkmcnt(10)\n\t"
      "v_mfma_f32_32x32x16_bf16 v[176:191], v[40:43], v[64:67], v[176:191]\n\t"
      "s_waitcnt lgkmcnt(9)\n\t"
      "v_mfma_f32_32x32x16_bf16 v[176:191], v[40:43], v[68:71], v[176:191]\n\t"
      "s_waitcnt lgkmcnt(8)\n\t"
      "v_mfma_f32_32x32x16_bf16 v[176:191], v[40:43], v[72:75], v[176:191]\n\t"
      "s_waitcnt lgkmcnt(5)\n\t"
      "v_mfma_f32_32x32x16_bf16 v[160:175], v[40:43], v[76:79], v[160:175]\n\t"
      "v_add_f32 v48, v48, v49\n\t"
      "v_add_f32 v50, v50, v51\n\t"
      "v_add_f32 v52, v52, v53\n\t"
      "v_add_f32 v54, v54, v55\n\t"
      "s_waitcnt lgkmcnt(4)\n\t"
      "v_mfma_f32_32x32x16_bf16 v[160:175], v[40:43], v[80:83], v[160:175]\n\t"
      "v_add_f32 v48, v48, v50\n\t"
      "v_add_f32 v52, v52, v54\n\t"
      "v_add_f32 v48, v48, v52\n\t"
      "v_rcp_f32 v48, v48\n\t"
      "s_waitcnt lgkmcnt(3)\n\t"
      "v_mfma_f32_32x32x16_bf16 v[160:175], v[40:43], v[84:87], v[160:175]\n\t"
      "s_nop 0\n\t"
      "v_mul_f32 v176, v48, v176\n\t"
      "v_mul_f32 v177, v48, v177\n\t"
      "v_mul_f32 v178, v48, v178\n\t"
      "v_mul_f32 v179, v48, v179\n\t"
      "s_waitcnt lgkmcnt(2)\n\t"
      "v_mfma_f32_32x32x16_bf16 v[160:175], v[44:47], v[88:91], v[160:175]\n\t"
      "v_mul_f32 v180, v48, v180\n\t"
      "v_mul_f32 v181, v48, v181\n\t"
      "v_mul_f32 v182, v48, v182\n\t"
      "v_mul_f32 v183, v48, v183\n\t"
      "v_cvt_pk_bf16_f32 v120, v176, v177\n\t"
      "s_waitcnt lgkmcnt(1)\n\t"
      "v_mfma_f32_32x32x16_bf16 v[160:175], v[44:47], v[92:95], v[160:175]\n\t"
      "v_cvt_pk_bf16_f32 v121, v178, v179\n\t"
      "v_cvt_pk_bf16_f32 v122, v180, v181\n\t"
      "v_cvt_pk_bf16_f32 v123, v182, v183\n\t"
      "s_waitcnt lgkmcnt(0)\n\t"
      "v_mfma_f32_32x32x16_bf16 v[160:175], v[44:47], v[96:99], v[160:175]\n\t"
      "ds_write_b128 v2, v[120:123] offset:2048\n\t"
      "s_nop 11\n\t"
      "v_mul_f32 v160, v48, v160\n\t"
      "v_mul_f32 v161, v48, v161\n\t"
      "v_mul_f32 v162, v48, v162\n\t"
      "v_mul_f32 v163, v48, v163\n\t"
      "v_mul_f32 v164, v48, v164\n\t"
      "v_mul_f32 v165, v48, v165\n\t"
      "v_mul_f32 v166, v48, v166\n\t"
      "v_mul_f32 v167, v48, v167\n\t"
      "v_cvt_pk_bf16_f32 v112, v160, v161\n\t"
      "v_cvt_pk_bf16_f32 v113, v162, v163\n\t"
      "v_cvt_pk_bf16_f32 v114, v164, v165\n\t"
      "v_cvt_pk_bf16_f32 v115, v166, v167\n\t"
      "ds_write_b128 v2, v[112:115] offset:0\n\t"
      "v_mul_f32 v168, v48, v168\n\t"
      "v_mul_f32 v169, v48, v169\n\t"
      "v_mul_f32 v170, v48, v170\n\t"
      "v_mul_f32 v171, v48, v171\n\t"
      "v_mul_f32 v172, v48, v172\n\t"
      "v_mul_f32 v173, v48, v173\n\t"
      "v_mul_f32 v174, v48, v174\n\t"
      "v_mul_f32 v175, v48, v175\n\t"
      "v_cvt_pk_bf16_f32 v116, v168, v169\n\t"
      "v_cvt_pk_bf16_f32 v117, v170, v171\n\t"
      "v_cvt_pk_bf16_f32 v118, v172, v173\n\t"
      "v_cvt_pk_bf16_f32 v119, v174, v175\n\t"
      "ds_write_b128 v2, v[116:119] offset:1024\n\t"
      "s_waitcnt lgkmcnt(0)\n\t"
      "s_barrier\n\t"
      "s_branch 29f\n\t"
      "21:\n\t"
      "s_waitcnt lgkmcnt(14)\n\t"
      "v_mfma_f32_32x32x16_bf16 v[160:175], v[64:67], v[16:19], v[160:175]\n\t"
      "v_add_f32 v0, v0, v26\n\t"
      "v_add_f32 v0, v0, v27\n\t"
      "v_add_f32 v0, v0, v28\n\t"
      "v_add_f32 v0, v0, v29\n\t"
      "v_add_f32 v0, v0, v30\n\t"
      "s_waitcnt lgkmcnt(12)\n\t"
      "v_mfma_f32_32x32x16_bf16 v[160:175], v[68:71], v[20:23], v[160:175]\n\t"
      "v_add_f32 v0, v0, v31\n\t"
      "v_add_f32 v0, v0, v8\n\t"
      "v_add_f32 v0, v0, v9\n\t"
      "v_add_f32 v0, v0, v10\n\t"
      "v_add_f32 v0, v0, v11\n\t"
      "v_mfma_f32_32x32x16_bf16 v[208:223], v[88:91], v[16:19], v[208:223]\n\t"
      "v_add_f32 v0, v0, v12\n\t"
      "v_add_f32 v0, v0, v13\n\t"
      "v_add_f32 v0, v0, v14\n\t"
      "v_add_f32 v0, v0, v15\n\t"
      "ds_write_b32 v7, v0 offset:8\n\t"
      "s_waitcnt lgkmcnt(1)\n\t"
      "v_mfma_f32_32x32x16_bf16 v[208:223], v[92:95], v[20:23], v[208:223]\n\t"
      "s_nop 4\n\t"
      "v_mfma_f32_32x32x16_bf16 v[224:239], v[96:99], v[16:19], v[224:239]\n\t"
      "v_cvt_pk_bf16_f32 v112, v160, v161\n\t"
      "v_cvt_pk_bf16_f32 v113, v162, v163\n\t"
      "v_cvt_pk_bf16_f32 v114, v164, v165\n\t"
      "v_cvt_pk_bf16_f32 v115, v166, v167\n\t"
      "ds_write_b128 v1, v[112:115] offset:12288\n\t"
      "v_mfma_f32_32x32x16_bf16 v[224:239], v[100:103], v[20:23], v[224:239]\n\t"
      "v_cvt_pk_bf16_f32 v116, v168, v169\n\t"
      "v_cvt_pk_bf16_f32 v117, v170, v171\n\t"
      "v_cvt_pk_bf16_f32 v118, v172, v173\n\t"
      "v_cvt_pk_bf16_f32 v119, v174, v175\n\t"
      "ds_write_b128 v1, v[116:119] offset:13312\n\t"
      "v_mfma_f32_32x32x16_bf16 v[240:255], v[104:107], v[16:19], v[240:255]\n\t"
      "v_cvt_pk_bf16_f32 v136, v208, v209\n\t"
      "v_cvt_pk_bf16_f32 v137, v210, v211\n\t"
      "v_cvt_pk_bf16_f32 v138, v212, v213\n\t"
      "v_cvt_pk_bf16_f32 v139, v214, v215\n\t"
      "ds_write_b128 v1, v[136:139] offset:18432\n\t"
      "v_mfma_f32_32x32x16_bf16 v[240:255], v[108:111], v[20:23], v[240:255]\n\t"
      "v_cvt_pk_bf16_f32 v140, v216, v217\n\t"
      "v_cvt_pk_bf16_f32 v141, v218, v219\n\t"
      "v_cvt_pk_bf16_f32 v142, v220, v221\n\t"
      "v_cvt_pk_bf16_f32 v143, v222, v223\n\t"
      "ds_write_b128 v1, v[140:143] offset:19456\n\t"
      "v_mfma_f32_32x32x16_bf16 v[176:191], v[72:75], v[16:19], v[176:191]\n\t"
      "v_cvt_pk_bf16_f32 v144, v224, v225\n\t"
      "v_cvt_pk_bf16_f32 v145, v226, v227\n\t"
      "v_cvt_pk_bf16_f32 v146, v228, v229\n\t"
      "v_cvt_pk_bf16_f32 v147, v230, v231\n\t"
      "ds_write_b128 v1, v[144:147] offset:20480\n\t"
      "v_mfma_f32_32x32x16_bf16 v[176:191], v[76:79], v[20:23], v[176:191]\n\t"
      "v_cvt_pk_bf16_f32 v148, v232, v233\n\t"
      "v_cvt_pk_bf16_f32 v149, v234, v235\n\t"
      "v_cvt_pk_bf16_f32 v150, v236, v237\n\t"
      "v_cvt_pk_bf16_f32 v151, v238, v239\n\t"
      "ds_write_b128 v1, v[148:151] offset:21504\n\t"
      "v_mfma_f32_32x32x16_bf16 v[192:207], v[80:83], v[16:19], v[192:207]\n\t"
      "v_cvt_pk_bf16_f32 v152, v240, v241\n\t"
      "v_cvt_pk_bf16_f32 v153, v242, v243\n\t"
      "v_cvt_pk_bf16_f32 v154, v244, v245\n\t"
      "v_cvt_pk_bf16_f32 v155, v246, v247\n\t"
      "ds_write_b128 v1, v[152:155] offset:22528\n\t"
      "v_mfma_f32_32x32x16_bf16 v[192:207], v[84:87], v[20:23], v[192:207]\n\t"
      "v_cvt_pk_bf16_f32 v156, v248, v249\n\t"
      "v_cvt_pk_bf16_f32 v157, v250, v251\n\t"
      "v_cvt_pk_bf16_f32 v158, v252, v253\n\t"
      "v_cvt_pk_bf16_f32 v159, v254, v255\n\t"
      "ds_write_b128 v1, v[156:159] offset:23552\n\t"
      "v_cvt_pk_bf16_f32 v120, v176, v177\n\t"
      "v_cvt_pk_bf16_f32 v121, v178, v179\n\t"
      "v_cvt_pk_bf16_f32 v122, v180, v181\n\t"
      "v_cvt_pk_bf16_f32 v123, v182, v183\n\t"
      "ds_write_b128 v1, v[120:123] offset:14336\n\t"
      "s_waitcnt vmcnt(0) lgkmcnt(0)\n\t"
      "s_barrier\n\t"
      "ds_read_b128 v[64:67], v1 offset:3072\n\t"
      "ds_read_b128 v[68:71], v1 offset:27648\n\t"
      "ds_read_b128 v[72:75], v1 offset:39936\n\t"
      "v_and_b32 v7, 0xffffffe0, v7\n\t"
      "ds_read_b128 v[48:51], v7\n\t"
      "ds_read_b128 v[52:55], v7 offset:16\n\t"
      "ds_read_b128 v[76:79], v1 offset:4096\n\t"
      "ds_read_b128 v[80:83], v1 offset:28672\n\t"
      "ds_read_b128 v[84:87], v1 offset:40960\n\t"
      "ds_read_b128 v[88:91], v1 offset:5120\n\t"
      "ds_read_b128 v[92:95], v1 offset:29696\n\t"
      "ds_read_b128 v[96:99], v1 offset:41984\n\t"
      "s_waitcnt lgkmcnt(10)\n\t"
      "v_mfma_f32_32x32x16_bf16 v[176:191], v[44:47], v[64:67], v[176:191]\n\t"
      "s_waitcnt lgkmcnt(9)\n\t"
      "v_mfma_f32_32x32x16_bf16 v[176:191], v[44:47], v[68:71], v[176:191]\n\t"
      "s_waitcnt lgkmcnt(8)\n\t"
      "v_mfma_f32_32x32x16_bf16 v[176:191], v[44:47], v[72:75], v[176:191]\n\t"
      "s_waitcnt lgkmcnt(5)\n\t"
      "v_mfma_f32_32x32x16_bf16 v[192:207], v[40:43], v[76:79], v[192:207]\n\t"
      "v_add_f32 v48, v48, v49\n\t"
      "v_add_f32 v50, v50, v51\n\t"
      "v_add_f32 v52, v52, v53\n\t"
      "v_add_f32 v54, v54, v55\n\t"
      "s_waitcnt lgkmcnt(4)\n\t"
      "v_mfma_f32_32x32x16_bf16 v[192:207], v[40:43], v[80:83], v[192:207]\n\t"
      "v_add_f32 v48, v48, v50\n\t"
      "v_add_f32 v52, v52, v54\n\t"
      "v_add_f32 v48, v48, v52\n\t"
      "v_rcp_f32 v48, v48\n\t"
      "s_waitcnt lgkmcnt(3)\n\t"
      "v_mfma_f32_32x32x16_bf16 v[192:207], v[40:43], v[84:87], v[192:207]\n\t"
      "s_nop 0\n\t"
      "v_mul_f32 v184, v48, v184\n\t"
      "v_mul_f32 v185, v48, v185\n\t"
      "v_mul_f32 v186, v48, v186\n\t"
      "v_mul_f32 v187, v48, v187\n\t"
      "s_waitcnt lgkmcnt(2)\n\t"
      "v_mfma_f32_32x32x16_bf16 v[192:207], v[44:47], v[88:91], v[192:207]\n\t"
      "v_mul_f32 v188, v48, v188\n\t"
      "v_mul_f32 v189, v48, v189\n\t"
      "v_mul_f32 v190, v48, v190\n\t"
      "v_mul_f32 v191, v48, v191\n\t"
      "v_cvt_pk_bf16_f32 v124, v184, v185\n\t"
      "s_waitcnt lgkmcnt(1)\n\t"
      "v_mfma_f32_32x32x16_bf16 v[192:207], v[44:47], v[92:95], v[192:207]\n\t"
      "v_cvt_pk_bf16_f32 v125, v186, v187\n\t"
      "v_cvt_pk_bf16_f32 v126, v188, v189\n\t"
      "v_cvt_pk_bf16_f32 v127, v190, v191\n\t"
      "s_waitcnt lgkmcnt(0)\n\t"
      "v_mfma_f32_32x32x16_bf16 v[192:207], v[44:47], v[96:99], v[192:207]\n\t"
      "ds_write_b128 v2, v[124:127] offset:3072\n\t"
      "s_nop 11\n\t"
      "v_mul_f32 v192, v48, v192\n\t"
      "v_mul_f32 v193, v48, v193\n\t"
      "v_mul_f32 v194, v48, v194\n\t"
      "v_mul_f32 v195, v48, v195\n\t"
      "v_mul_f32 v196, v48, v196\n\t"
      "v_mul_f32 v197, v48, v197\n\t"
      "v_mul_f32 v198, v48, v198\n\t"
      "v_mul_f32 v199, v48, v199\n\t"
      "v_cvt_pk_bf16_f32 v128, v192, v193\n\t"
      "v_cvt_pk_bf16_f32 v129, v194, v195\n\t"
      "v_cvt_pk_bf16_f32 v130, v196, v197\n\t"
      "v_cvt_pk_bf16_f32 v131, v198, v199\n\t"
      "ds_write_b128 v2, v[128:131] offset:4096\n\t"
      "v_mul_f32 v200, v48, v200\n\t"
      "v_mul_f32 v201, v48, v201\n\t"
      "v_mul_f32 v202, v48, v202\n\t"
      "v_mul_f32 v203, v48, v203\n\t"
      "v_mul_f32 v204, v48, v204\n\t"
      "v_mul_f32 v205, v48, v205\n\t"
      "v_mul_f32 v206, v48, v206\n\t"
      "v_mul_f32 v207, v48, v207\n\t"
      "v_cvt_pk_bf16_f32 v132, v200, v201\n\t"
      "v_cvt_pk_bf16_f32 v133, v202, v203\n\t"
      "v_cvt_pk_bf16_f32 v134, v204, v205\n\t"
      "v_cvt_pk_bf16_f32 v135, v206, v207\n\t"
      "ds_write_b128 v2, v[132:135] offset:5120\n\t"
      "s_waitcnt lgkmcnt(0)\n\t"
      "s_barrier\n\t"
      "s_branch 29f\n\t"
      "22:\n\t"
      "s_waitcnt lgkmcnt(14)\n\t"
      "v_mfma_f32_32x32x16_bf16 v[160:175], v[64:67], v[16:19], v[160:175]\n\t"
      "v_add_f32 v0, v0, v26\n\t"
      "v_add_f32 v0, v0, v27\n\t"
      "v_add_f32 v0, v0, v28\n\t"
      "v_add_f32 v0, v0, v29\n\t"
      "v_add_f32 v0, v0, v30\n\t"
      "s_waitcnt lgkmcnt(12)\n\t"
      "v_mfma_f32_32x32x16_bf16 v[160:175], v[68:71], v[20:23], v[160:175]\n\t"
      "v_add_f32 v0, v0, v31\n\t"
      "v_add_f32 v0, v0, v8\n\t"
      "v_add_f32 v0, v0, v9\n\t"
      "v_add_f32 v0, v0, v10\n\t"
      "v_add_f32 v0, v0, v11\n\t"
      "v_mfma_f32_32x32x16_bf16 v[176:191], v[72:75], v[16:19], v[176:191]\n\t"
      "v_add_f32 v0, v0, v12\n\t"
      "v_add_f32 v0, v0, v13\n\t"
      "v_add_f32 v0, v0, v14\n\t"
      "v_add_f32 v0, v0, v15\n\t"
      "ds_write_b32 v7, v0 offset:16\n\t"
      "s_waitcnt lgkmcnt(1)\n\t"
      "v_mfma_f32_32x32x16_bf16 v[176:191], v[76:79], v[20:23], v[176:191]\n\t"
      "s_nop 4\n\t"
      "v_mfma_f32_32x32x16_bf16 v[192:207], v[80:83], v[16:19], v[192:207]\n\t"
      "v_cvt_pk_bf16_f32 v112, v160, v161\n\t"
      "v_cvt_pk_bf16_f32 v113, v162, v163\n\t"
      "v_cvt_pk_bf16_f32 v114, v164, v165\n\t"
      "v_cvt_pk_bf16_f32 v115, v166, v167\n\t"
      "ds_write_b128 v1, v[112:115] offset:24576\n\t"
      "v_mfma_f32_32x32x16_bf16 v[192:207], v[84:87], v[20:23], v[192:207]\n\t"
      "v_cvt_pk_bf16_f32 v116, v168, v169\n\t"
      "v_cvt_pk_bf16_f32 v117, v170, v171\n\t"
      "v_cvt_pk_bf16_f32 v118, v172, v173\n\t"
      "v_cvt_pk_bf16_f32 v119, v174, v175\n\t"
      "ds_write_b128 v1, v[116:119] offset:25600\n\t"
      "v_mfma_f32_32x32x16_bf16 v[240:255], v[104:107], v[16:19], v[240:255]\n\t"
      "v_cvt_pk_bf16_f32 v120, v176, v177\n\t"
      "v_cvt_pk_bf16_f32 v121, v178, v179\n\t"
      "v_cvt_pk_bf16_f32 v122, v180, v181\n\t"
      "v_cvt_pk_bf16_f32 v123, v182, v183\n\t"
      "ds_write_b128 v1, v[120:123] offset:26624\n\t"
      "v_mfma_f32_32x32x16_bf16 v[240:255], v[108:111], v[20:23], v[240:255]\n\t"
      "v_cvt_pk_bf16_f32 v124, v184, v185\n\t"
      "v_cvt_pk_bf16_f32 v125, v186, v187\n\t"
      "v_cvt_pk_bf16_f32 v126, v188, v189\n\t"
      "v_cvt_pk_bf16_f32 v127, v190, v191\n\t"
      "ds_write_b128 v1, v[124:127] offset:27648\n\t"
      "v_mfma_f32_32x32x16_bf16 v[224:239], v[96:99], v[16:19], v[224:239]\n\t"
      "v_cvt_pk_bf16_f32 v128, v192, v193\n\t"
      "v_cvt_pk_bf16_f32 v129, v194, v195\n\t"
      "v_cvt_pk_bf16_f32 v130, v196, v197\n\t"
      "v_cvt_pk_bf16_f32 v131, v198, v199\n\t"
      "ds_write_b128 v1, v[128:131] offset:28672\n\t"
      "v_mfma_f32_32x32x16_bf16 v[224:239], v[100:103], v[20:23], v[224:239]\n\t"
      "v_cvt_pk_bf16_f32 v132, v200, v201\n\t"
      "v_cvt_pk_bf16_f32 v133, v202, v203\n\t"
      "v_cvt_pk_bf16_f32 v134, v204, v205\n\t"
      "v_cvt_pk_bf16_f32 v135, v206, v207\n\t"
      "ds_write_b128 v1, v[132:135] offset:29696\n\t"
      "v_mfma_f32_32x32x16_bf16 v[208:223], v[88:91], v[16:19], v[208:223]\n\t"
      "v_cvt_pk_bf16_f32 v152, v240, v241\n\t"
      "v_cvt_pk_bf16_f32 v153, v242, v243\n\t"
      "v_cvt_pk_bf16_f32 v154, v244, v245\n\t"
      "v_cvt_pk_bf16_f32 v155, v246, v247\n\t"
      "ds_write_b128 v1, v[152:155] offset:34816\n\t"
      "v_mfma_f32_32x32x16_bf16 v[208:223], v[92:95], v[20:23], v[208:223]\n\t"
      "v_cvt_pk_bf16_f32 v156, v248, v249\n\t"
      "v_cvt_pk_bf16_f32 v157, v250, v251\n\t"
      "v_cvt_pk_bf16_f32 v158, v252, v253\n\t"
      "v_cvt_pk_bf16_f32 v159, v254, v255\n\t"
      "ds_write_b128 v1, v[156:159] offset:35840\n\t"
      "v_cvt_pk_bf16_f32 v148, v232, v233\n\t"
      "v_cvt_pk_bf16_f32 v149, v234, v235\n\t"
      "v_cvt_pk_bf16_f32 v150, v236, v237\n\t"
      "v_cvt_pk_bf16_f32 v151, v238, v239\n\t"
      "ds_write_b128 v1, v[148:151] offset:33792\n\t"
      "s_waitcnt vmcnt(0) lgkmcnt(0)\n\t"
      "s_barrier\n\t"
      "ds_read_b128 v[64:67], v1 offset:8192\n\t"
      "ds_read_b128 v[68:71], v1 offset:20480\n\t"
      "ds_read_b128 v[72:75], v1 offset:45056\n\t"
      "v_and_b32 v7, 0xffffffe0, v7\n\t"
      "ds_read_b128 v[48:51], v7\n\t"
      "ds_read_b128 v[52:55], v7 offset:16\n\t"
      "ds_read_b128 v[76:79], v1 offset:6144\n\t"
      "ds_read_b128 v[80:83], v1 offset:18432\n\t"
      "ds_read_b128 v[84:87], v1 offset:43008\n\t"
      "ds_read_b128 v[88:91], v1 offset:7168\n\t"
      "ds_read_b128 v[92:95], v1 offset:19456\n\t"
      "ds_read_b128 v[96:99], v1 offset:44032\n\t"
      "s_waitcnt lgkmcnt(10)\n\t"
      "v_mfma_f32_32x32x16_bf16 v[224:239], v[40:43], v[64:67], v[224:239]\n\t"
      "s_waitcnt lgkmcnt(9)\n\t"
      "v_mfma_f32_32x32x16_bf16 v[224:239], v[40:43], v[68:71], v[224:239]\n\t"
      "s_waitcnt lgkmcnt(8)\n\t"
      "v_mfma_f32_32x32x16_bf16 v[224:239], v[40:43], v[72:75], v[224:239]\n\t"
      "s_waitcnt lgkmcnt(5)\n\t"
      "v_mfma_f32_32x32x16_bf16 v[208:223], v[40:43], v[76:79], v[208:223]\n\t"
      "v_add_f32 v48, v48, v49\n\t"
      "v_add_f32 v50, v50, v51\n\t"
      "v_add_f32 v52, v52, v53\n\t"
      "v_add_f32 v54, v54, v55\n\t"
      "s_waitcnt lgkmcnt(4)\n\t"
      "v_mfma_f32_32x32x16_bf16 v[208:223], v[40:43], v[80:83], v[208:223]\n\t"
      "v_add_f32 v48, v48, v50\n\t"
      "v_add_f32 v52, v52, v54\n\t"
      "v_add_f32 v48, v48, v52\n\t"
      "v_rcp_f32 v48, v48\n\t"
      "s_waitcnt lgkmcnt(3)\n\t"
      "v_mfma_f32_32x32x16_bf16 v[208:223], v[40:43], v[84:87], v[208:223]\n\t"
      "s_nop 0\n\t"
      "v_mul_f32 v224, v48, v224\n\t"
      "v_mul_f32 v225, v48, v225\n\t"
      "v_mul_f32 v226, v48, v226\n\t"
      "v_mul_f32 v227, v48, v227\n\t"
      "s_waitcnt lgkmcnt(2)\n\t"
      "v_mfma_f32_32x32x16_bf16 v[208:223], v[44:47], v[88:91], v[208:223]\n\t"
      "v_mul_f32 v228, v48, v228\n\t"
      "v_mul_f32 v229, v48, v229\n\t"
      "v_mul_f32 v230, v48, v230\n\t"
      "v_mul_f32 v231, v48, v231\n\t"
      "v_cvt_pk_bf16_f32 v144, v224, v225\n\t"
      "s_waitcnt lgkmcnt(1)\n\t"
      "v_mfma_f32_32x32x16_bf16 v[208:223], v[44:47], v[92:95], v[208:223]\n\t"
      "v_cvt_pk_bf16_f32 v145, v226, v227\n\t"
      "v_cvt_pk_bf16_f32 v146, v228, v229\n\t"
      "v_cvt_pk_bf16_f32 v147, v230, v231\n\t"
      "s_waitcnt lgkmcnt(0)\n\t"
      "v_mfma_f32_32x32x16_bf16 v[208:223], v[44:47], v[96:99], v[208:223]\n\t"
      "ds_write_b128 v2, v[144:147] offset:8192\n\t"
      "s_nop 11\n\t"
      "v_mul_f32 v208, v48, v208\n\t"
      "v_mul_f32 v209, v48, v209\n\t"
      "v_mul_f32 v210, v48, v210\n\t"
      "v_mul_f32 v211, v48, v211\n\t"
      "v_mul_f32 v212, v48, v212\n\t"
      "v_mul_f32 v213, v48, v213\n\t"
      "v_mul_f32 v214, v48, v214\n\t"
      "v_mul_f32 v215, v48, v215\n\t"
      "v_cvt_pk_bf16_f32 v136, v208, v209\n\t"
      "v_cvt_pk_bf16_f32 v137, v210, v211\n\t"
      "v_cvt_pk_bf16_f32 v138, v212, v213\n\t"
      "v_cvt_pk_bf16_f32 v139, v214, v215\n\t"
      "ds_write_b128 v2, v[136:139] offset:6144\n\t"
      "v_mul_f32 v216, v48, v216\n\t"
      "v_mul_f32 v217, v48, v217\n\t"
      "v_mul_f32 v218, v48, v218\n\t"
      "v_mul_f32 v219, v48, v219\n\t"
      "v_mul_f32 v220, v48, v220\n\t"
      "v_mul_f32 v221, v48, v221\n\t"
      "v_mul_f32 v222, v48, v222\n\t"
      "v_mul_f32 v223, v48, v223\n\t"
      "v_cvt_pk_bf16_f32 v140, v216, v217\n\t"
      "v_cvt_pk_bf16_f32 v141, v218, v219\n\t"
      "v_cvt_pk_bf16_f32 v142, v220, v221\n\t"
      "v_cvt_pk_bf16_f32 v143, v222, v223\n\t"
      "ds_write_b128 v2, v[140:143] offset:7168\n\t"
      "s_waitcnt lgkmcnt(0)\n\t"
      "s_barrier\n\t"
      "29:\n\t"
      "s_branch 32f\n\t"
      "63:\n\t"
      "s_sleep 2\n\t"
      "buffer_load_dwordx2 v[28:29], v15, %[rs], 0 offen sc1\n\t"
      "s_waitcnt vmcnt(0)\n\t"
      "v_cmp_ne_u64 vcc, %[tag], v[28:29]\n\t"
      "s_cbranch_vccz 64b\n\t"
      "s_sub_u32 %[cnt], %[cnt], 1\n\t"
      "s_cbranch_scc0 63b\n\t"
      "s_branch 64b\n\t"
      "30:\n\t"
      "v_subrev_u32 v1, %[grp], v1\n\t"
      "v_subrev_u32 v2, %[grp], v2\n\t"
      "v_mov_b32 v9, v1\n\t"
      "v_mov_b32 v10, v2\n\t"
      "v_mov_b32 v6, 0xff800000\n\t"
      "s_mov_b32 %[cnt], 7\n\t"
      "33:\n\t"
      "ds_read_b128 v[64:67], v9 offset:0\n\t"
      "ds_read_b128 v[68:71], v10 offset:1024\n\t"
      "ds_read_b128 v[72:75], v9 offset:2048\n\t"
      "ds_read_b128 v[76:79], v10 offset:3072\n\t"
      "ds_read_b128 v[80:83], v9 offset:4096\n\t"
      "ds_read_b128 v[84:87], v10 offset:5120\n\t"
      "ds_read_b128 v[88:91], v9 offset:6144\n\t"
      "ds_read_b128 v[92:95], v10 offset:7168\n\t"
      "ds_read_b128 v[96:99], v9 offset:8192\n\t"
      "ds_read_b128 v[100:103], v10 offset:9216\n\t"
      "ds_read_b128 v[104:107], v9 offset:10240\n\t"
      "ds_read_b128 v[108:111], v10 offset:11264\n\t"
      "s_bitcmp1_b32 %[msk], %[cnt]\n\t"
      "s_cselect_b32 %[dl], 0xfffe8000, %[grp]\n\t"
      "s_waitcnt lgkmcnt(0)\n\t"
      "v_mfma_f32_32x32x16_bf16 v[24:39], v[64:67], v[112:115], 0\n\t"
      "v_mfma_f32_32x32x16_bf16 v[24:39], v[68:71], v[116:119], v[24:39]\n\t"
      "v_mfma_f32_32x32x16_bf16 v[24:39], v[72:75], v[120:123], v[24:39]\n\t"
      "v_mfma_f32_32x32x16_bf16 v[24:39], v[76:79], v[124:127], v[24:39]\n\t"
      "v_mfma_f32_32x32x16_bf16 v[24:39], v[80:83], v[128:131], v[24:39]\n\t"
      "v_mfma_f32_32x32x16_bf16 v[24:39], v[84:87], v[132:135], v[24:39]\n\t"
      "v_mfma_f32_32x32x16_bf16 v[24:39], v[88:91], v[136:139], v[24:39]\n\t"
      "v_mfma_f32_32x32x16_bf16 v[24:39], v[92:95], v[140:143], v[24:39]\n\t"
      "v_mfma_f32_32x32x16_bf16 v[24:39], v[96:99], v[144:147], v[24:39]\n\t"
      "v_mfma_f32_32x32x16_bf16 v[24:39], v[100:103], v[148:151], v[24:39]\n\t"
      "v_mfma_f32_32x32x16_bf16 v[24:39], v[104:107], v[152:155], v[24:39]\n\t"
      "v_mfma_f32_32x32x16_bf16 v[24:39], v[108:111], v[156:159], v[24:39]\n\t"
      "v_add_u32 v9, %[dl], v9\n\t"
      "v_add_u32 v10, %[dl], v10\n\t"
      "s_nop 11\n\t"
      "v_max3_f32 v6, v6, v24, v25\n\t"
      "v_max3_f32 v6, v6, v26, v27\n\t"
      "v_max3_f32 v6, v6, v28, v29\n\t"
      "v_max3_f32 v6, v6, v30, v31\n\t"
      "v_max3_f32 v6, v6, v32, v33\n\t"
      "v_max3_f32 v6, v6, v34, v35\n\t"
      "v_max3_f32 v6, v6, v36, v37\n\t"
      "v_max3_f32 v6, v6, v38, v39\n\t"
      "s_sub_u32 %[cnt], %[cnt], 1\n\t"
      "s_barrier\n\t"
      "s_cbranch_scc0 33b\n\t"
      "v_mov_b32 v9, v6\n\t"
      "v_and_b32 v10, 0x1f0, v5\n\t"
      "v_add_u32 v10, 161792, v10\n\t"
      "v_permlane32_swap_b32 v6, v9\n\t"
      "s_lshl_b32 %[dl], %[w], 2\n\t"
      "v_max_f32 v6, v6, v9\n\t"
      "v_add_u32 v11, %[dl], v10\n\t"
      "ds_write_b32 v11, v6\n\t"
      "s_waitcnt lgkmcnt(0)\n\t"
      "s_barrier\n\t"
      "ds_read_b128 v[64:67], v10\n\t"
      "ds_read_b128 v[40:43], v1 offset:0\n\t"
      "ds_read_b128 v[44:47], v2 offset:1024\n\t"
      "ds_read_b128 v[48:51], v1 offset:2048\n\t"
      "ds_read_b128 v[52:55], v2 offset:3072\n\t"
      "ds_read_b128 v[56:59], v1 offset:4096\n\t"
      "ds_read_b128 v[60:63], v2 offset:5120\n\t"
      "s_mov_b32 %[cnt], 6\n\t"
      "s_mov_b32 %[dl], %[grp]\n\t"
      "s_waitcnt lgkmcnt(0)\n\t"
      "v_max3_f32 v64, v64, v65, v66\n\t"
      "v_max_f32 v64, v64, v67\n\t"
      "v_sub_f32 v6, 0, v64\n\t"
      "v_mfma_f32_32x32x16_bf16 v[24:39], v[40:43], v[112:115], 0\n\t"
      "ds_read_b128 v[40:43], v1 offset:6144\n\t"
      "v_mfma_f32_32x32x16_bf16 v[24:39], v[44:47], v[116:119], v[24:39]\n\t"
      "ds_read_b128 v[44:47], v2 offset:7168\n\t"
      "v_mfma_f32_32x32x16_bf16 v[24:39], v[48:51], v[120:123], v[24:39]\n\t"
      "ds_read_b128 v[48:51], v1 offset:8192\n\t"
      "v_mfma_f32_32x32x16_bf16 v[24:39], v[52:55], v[124:127], v[24:39]\n\t"
      "ds_read_b128 v[52:55], v2 offset:9216\n\t"
      "v_mfma_f32_32x32x16_bf16 v[24:39], v[56:59], v[128:131], v[24:39]\n\t"
      "ds_read_b128 v[56:59], v1 offset:10240\n\t"
      "v_mfma_f32_32x32x16_bf16 v[24:39], v[60:63], v[132:135], v[24:39]\n\t"
      "ds_read_b128 v[60:63], v2 offset:11264\n\t"
      "s_waitcnt lgkmcnt(5)\n\t"
      "v_mfma_f32_32x32x16_bf16 v[24:39], v[40:43], v[136:139], v[24:39]\n\t"
      "v_add_u32 v1, %[grp], v1\n\t"
      "v_add_u32 v2, %[grp], v2\n\t"
      "s_waitcnt lgkmcnt(4)\n\t"
      "v_mfma_f32_32x32x16_bf16 v[24:39], v[44:47], v[140:143], v[24:39]\n\t"
      "s_waitcnt lgkmcnt(3)\n\t"
      "v_mfma_f32_32x32x16_bf16 v[24:39], v[48:51], v[144:147], v[24:39]\n\t"
      "s_waitcnt lgkmcnt(2)\n\t"
      "v_mfma_f32_32x32x16_bf16 v[24:39], v[52:55], v[148:151], v[24:39]\n\t"
      "s_waitcnt lgkmcnt(1)\n\t"
      "v_mfma_f32_32x32x16_bf16 v[24:39], v[56:59], v[152:155], v[24:39]\n\t"
      "s_waitcnt lgkmcnt(0)\n\t"
      "v_mfma_f32_32x32x16_bf16 v[24:39], v[60:63], v[156:159], v[24:39]\n\t"
      "s_barrier\n\t"
      "v_mfma_f32_32x32x16_bf16 v[160:175], v[20:23], v[20:23], 0\n\t"
      "s_nop 0\n\t"
      "ds_read_b128 v[40:43], v1 offset:0\n\t"
      "ds_read_b128 v[44:47], v2 offset:1024\n\t"
      "s_nop 2\n\t"
      "v_mfma_f32_32x32x16_bf16 v[176:191], v[20:23], v[20:23], 0\n\t"
      "ds_read_b128 v[48:51], v1 offset:2048\n\t"
      "ds_read_b128 v[52:55], v2 offset:3072\n\t"
      "s_nop 0\n\t"
      "v_add_f32 v24, v6, v24\n\t"
      "v_add_f32 v25, v6, v25\n\t"
      "v_add_f32 v26, v6, v26\n\t"
      "v_add_f32 v27, v6, v27\n\t"
      "v_add_f32 v28, v6, v28\n\t"
      "v_add_f32 v29, v6, v29\n\t"
      "v_add_f32 v30, v6, v30\n\t"
      "v_add_f32 v31, v6, v31\n\t"
      "v_add_f32 v32, v6, v32\n\t"
      "v_add_f32 v33, v6, v33\n\t"
      "v_add_f32 v34, v6, v34\n\t"
      "v_add_f32 v35, v6, v35\n\t"
      "v_add_f32 v36, v6, v36\n\t"
      "v_add_f32 v37, v6, v37\n\t"
      "v_add_f32 v38, v6, v38\n\t"
      "v_add_f32 v39, v6, v39\n\t"
      "v_exp_f32 v24, v24\n\t"
      "v_mfma_f32_32x32x16_bf16 v[192:207], v[20:23], v[20:23], 0\n\t"
      "v_exp_f32 v25, v25\n\t"
      "v_exp_f32 v26, v26\n\t"
      "ds_read_b128 v[56:59], v1 offset:4096\n\t"
      "v_mfma_f32_32x32x16_bf16 v[208:223], v[20:23], v[20:23], 0\n\t"
      "v_exp_f32 v27, v27\n\t"
      "v_exp_f32 v28, v28\n\t"
      "ds_read_b128 v[60:63], v2 offset:5120\n\t"
      "v_mfma_f32_32x32x16_bf16 v[224:239], v[20:23], v[20:23], 0\n\t"
      "v_exp_f32 v29, v29\n\t"
      "v_exp_f32 v30, v30\n\t"
      "v_add_f32 v0, v0, v24\n\t"
      "v_mfma_f32_32x32x16_bf16 v[240:255], v[20:23], v[20:23], 0\n\t"
      "v_exp_f32 v31, v31\n\t"
      "v_exp_f32 v8, v32\n\t"
      "v_add_f32 v0, v0, v25\n\t"
      "v_exp_f32 v9, v33\n\t"
      "v_exp_f32 v10, v34\n\t"
      "v_cvt_pk_bf16_f32 v16, v24, v25\n\t"
      "v_exp_f32 v11, v35\n\t"
      "v_exp_f32 v12, v36\n\t"
      "v_cvt_pk_bf16_f32 v17, v26, v27\n\t"
      "v_exp_f32 v13, v37\n\t"
      "v_exp_f32 v14, v38\n\t"
      "v_cvt_pk_bf16_f32 v18, v28, v29\n\t"
      "v_exp_f32 v15, v39\n\t"
      "v_cvt_pk_bf16_f32 v19, v30, v31\n\t"
      "v_add_f32 v0, v0, v26\n\t"
      "v_add_f32 v0, v0, v27\n\t"
      "v_add_f32 v0, v0, v28\n\t"
      "v_add_f32 v0, v0, v29\n\t"
      "v_add_f32 v0, v0, v30\n\t"
      "v_add_f32 v0, v0, v31\n\t"
      "s_waitcnt lgkmcnt(0)\n\t"
      "s_branch 4f\n\t"
      "1:\n\t"
      "v_mfma_f32_32x32x16_bf16 v[240:255], v[108:111], v[20:23], v[240:255]\n\t"
      "4:\n\t"
      "s_barrier\n\t"
      "v_mfma_f32_32x32x16_bf16 v[24:39], v[40:43], v[112:115], 0\n\t"
      "ds_read_b128 v[40:43], v1 offset:6144\n\t"
      "ds_read_b64_tr_b16 v[64:65], v3 offset:0\n\t"
      "ds_read_b64_tr_b16 v[66:67], v3 offset:256\n\t"
      "v_cvt_pk_bf16_f32 v20, v8, v9\n\t"
      "v_cvt_pk_bf16_f32 v21, v10, v11\n\t"
      "v_mfma_f32_32x32x16_bf16 v[24:39], v[44:47], v[116:119], v[24:39]\n\t"
      "ds_read_b128 v[44:47], v2 offset:7168\n\t"
      "ds_read_b64_tr_b16 v[72:73], v3 offset:2048\n\t"
      "ds_read_b64_tr_b16 v[74:75], v3 offset:2304\n\t"
      "v_cvt_pk_bf16_f32 v22, v12, v13\n\t"
      "v_cvt_pk_bf16_f32 v23, v14, v15\n\t"
      "v_mfma_f32_32x32x16_bf16 v[24:39], v[48:51], v[120:123], v[24:39]\n\t"
      "ds_read_b128 v[48:51], v1 offset:8192\n\t"
      "ds_read_b64_tr_b16 v[80:81], v3 offset:4096\n\t"
      "ds_read_b64_tr_b16 v[82:83], v3 offset:4352\n\t"
      "v_add_f32 v0, v0, v8\n\t"
      "v_add_f32 v0, v0, v9\n\t"
      "v_mfma_f32_32x32x16_bf16 v[24:39], v[52:55], v[124:127], v[24:39]\n\t"
      "ds_read_b128 v[52:55], v2 offset:9216\n\t"
      "ds_read_b64_tr_b16 v[88:89], v3 offset:6144\n\t"
      "ds_read_b64_tr_b16 v[90:91], v3 offset:6400\n\t"
      "v_add_f32 v0, v0, v10\n\t"
      "v_add_f32 v0, v0, v11\n\t"
      "v_mfma_f32_32x32x16_bf16 v[24:39], v[56:59], v[128:131], v[24:39]\n\t"
      "ds_read_b128 v[56:59], v1 offset:10240\n\t"
      "ds_read_b64_tr_b16 v[96:97], v3 offset:8192\n\t"
      "ds_read_b64_tr_b16 v[98:99], v3 offset:8448\n\t"
      "v_add_f32 v0, v0, v12\n\t"
      "v_add_f32 v0, v0, v13\n\t"
      "v_mfma_f32_32x32x16_bf16 v[24:39], v[60:63], v[132:135], v[24:39]\n\t"
      "ds_read_b128 v[60:63], v2 offset:11264\n\t"
      "ds_read_b64_tr_b16 v[104:105], v3 offset:10240\n\t"
      "ds_read_b64_tr_b16 v[106:107], v3 offset:10496\n\t"
      "v_add_f32 v0, v0, v14\n\t"
      "s_waitcnt lgkmcnt(14)\n\t"
      "v_mfma_f32_32x32x16_bf16 v[24:39], v[40:43], v[136:139], v[24:39]\n\t"
      "ds_read_b64_tr_b16 v[68:69], v4 offset:0\n\t"
      "ds_read_b64_tr_b16 v[70:71], v4 offset:256\n\t"
      "ds_read_b64_tr_b16 v[76:77], v4 offset:2048\n\t"
      "ds_read_b64_tr_b16 v[78:79], v4 offset:2304\n\t"
      "v_add_u32 v3, %[dl], v3\n\t"
      "v_mfma_f32_32x32x16_bf16 v[24:39], v[44:47], v[140:143], v[24:39]\n\t"
      "ds_read_b64_tr_b16 v[84:85], v4 offset:4096\n\t"
      "ds_read_b64_tr_b16 v[86:87], v4 offset:4352\n\t"
      "ds_read_b64_tr_b16 v[92:93], v4 offset:6144\n\t"
      "ds_read_b64_tr_b16 v[94:95], v4 offset:6400\n\t"
      "s_waitcnt lgkmcnt(15)\n\t"
      "v_mfma_f32_32x32x16_bf16 v[24:39], v[48:51], v[144:147], v[24:39]\n\t"
      "ds_read_b64_tr_b16 v[100:101], v4 offset:8192\n\t"
      "ds_read_b64_tr_b16 v[102:103], v4 offset:8448\n\t"
      "ds_read_b64_tr_b16 v[108:109], v4 offset:10240\n\t"
      "ds_read_b64_tr_b16 v[110:111], v4 offset:10496\n\t"
      "v_mfma_f32_32x32x16_bf16 v[24:39], v[52:55], v[148:151], v[24:39]\n\t"
      "v_add_f32 v0, v0, v15\n\t"
      "v_add_u32 v4, %[dl], v4\n\t"
      "s_bitcmp1_b32 %[msk], %[cnt]\n\t"
      "s_waitcnt lgkmcnt(14)\n\t"
      "v_mfma_f32_32x32x16_bf16 v[24:39], v[56:59], v[152:155], v[24:39]\n\t"
      "s_cselect_b32 %[dl], 0xfffe8000, %[grp]\n\t"
      "v_add_u32 v1, %[dl], v1\n\t"
      "v_add_u32 v2, %[dl], v2\n\t"
      "s_sub_u32 %[cnt], %[cnt], 1\n\t"
      "v_mfma_f32_32x32x16_bf16 v[24:39], v[60:63], v[156:159], v[24:39]\n\t"
      "s_waitcnt lgkmcnt(0)\n\t"
      "s_barrier\n\t"
      "s_cbranch_scc1 5f\n\t"
      "v_mfma_f32_32x32x16_bf16 v[160:175], v[64:67], v[16:19], v[160:175]\n\t"
      "ds_read_b128 v[40:43], v1 offset:0\n\t"
      "ds_read_b128 v[44:47], v2 offset:1024\n\t"
      "s_nop 1\n\t"
      "v_mfma_f32_32x32x16_bf16 v[176:191], v[72:75], v[16:19], v[176:191]\n\t"
      "ds_read_b128 v[48:51], v1 offset:2048\n\t"
      "ds_read_b128 v[52:55], v2 offset:3072\n\t"
      "s_nop 0\n\t"
      "v_add_f32 v24, v6, v24\n\t"
      "v_add_f32 v25, v6, v25\n\t"
      "v_add_f32 v26, v6, v26\n\t"
      "v_add_f32 v27, v6, v27\n\t"
      "v_add_f32 v28, v6, v28\n\t"
      "v_add_f32 v29, v6, v29\n\t"
      "v_add_f32 v30, v6, v30\n\t"
      "v_add_f32 v31, v6, v31\n\t"
      "v_add_f32 v32, v6, v32\n\t"
      "v_add_f32 v33, v6, v33\n\t"
      "v_add_f32 v34, v6, v34\n\t"
      "v_add_f32 v35, v6, v35\n\t"
      "v_add_f32 v36, v6, v36\n\t"
      "v_add_f32 v37, v6, v37\n\t"
      "v_add_f32 v38, v6, v38\n\t"
      "v_add_f32 v39, v6, v39\n\t"
      "v_exp_f32 v24, v24\n\t"
      "v_mfma_f32_32x32x16_bf16 v[192:207], v[80:83], v[16:19], v[192:207]\n\t"
      "v_exp_f32 v25, v25\n\t"
      "v_exp_f32 v26, v26\n\t"
      "ds_read_b128 v[56:59], v1 offset:4096\n\t"
      "v_mfma_f32_32x32x16_bf16 v[208:223], v[88:91], v[16:19], v[208:223]\n\t"
      "v_exp_f32 v27, v27\n\t"
      "v_exp_f32 v28, v28\n\t"
      "ds_read_b128 v[60:63], v2 offset:5120\n\t"
      "v_mfma_f32_32x32x16_bf16 v[224:239], v[96:99], v[16:19], v[224:239]\n\t"
      "v_exp_f32 v29, v29\n\t"
      "v_exp_f32 v30, v30\n\t"
      "v_add_f32 v0, v0, v24\n\t"
      "v_mfma_f32_32x32x16_bf16 v[240:255], v[104:107], v[16:19], v[240:255]\n\t"
      "v_exp_f32 v31, v31\n\t"
      "v_exp_f32 v8, v32\n\t"
      "v_add_f32 v0, v0, v25\n\t"
      "v_mfma_f32_32x32x16_bf16 v[160:175], v[68:71], v[20:23], v[160:175]\n\t"
      "v_exp_f32 v9, v33\n\t"
      "v_exp_f32 v10, v34\n\t"
      "v_cvt_pk_bf16_f32 v16, v24, v25\n\t"
      "v_mfma_f32_32x32x16_bf16 v[176:191], v[76:79], v[20:23], v[176:191]\n\t"
      "v_exp_f32 v11, v35\n\t"
      "v_exp_f32 v12, v36\n\t"
      "v_cvt_pk_bf16_f32 v17, v26, v27\n\t"
      "v_mfma_f32_32x32x16_bf16 v[192:207], v[84:87], v[20:23], v[192:207]\n\t"
      "v_exp_f32 v13, v37\n\t"
      "v_exp_f32 v14, v38\n\t"
      "v_cvt_pk_bf16_f32 v18, v28, v29\n\t"
      "v_mfma_f32_32x32x16_bf16 v[208:223], v[92:95], v[20:23], v[208:223]\n\t"
      "v_exp_f32 v15, v39\n\t"
      "v_cvt_pk_bf16_f32 v19, v30, v31\n\t"
      "v_add_f32 v0, v0, v26\n\t"
      "v_add_f32 v0, v0, v27\n\t"
      "v_mfma_f32_32x32x16_bf16 v[224:239], v[100:103], v[20:23], v[224:239]\n\t"
      "v_add_f32 v0, v0, v28\n\t"
      "v_add_f32 v0, v0, v29\n\t"
      "v_add_f32 v0, v0, v30\n\t"
      "v_add_f32 v0, v0, v31\n\t"
      "s_waitcnt lgkmcnt(0)\n\t"
      "s_branch 1b\n\t"
      "5:\n\t"
      "v_mfma_f32_32x32x16_bf16 v[160:175], v[64:67], v[16:19], v[160:175]\n\t"
      "ds_read_b64_tr_b16 v[64:65], v3 offset:0\n\t"
      "ds_read_b64_tr_b16 v[66:67], v3 offset:256\n\t"
      "v_add_u32 v1, 98304, v5\n\t"
      "v_add_u32 v2, 147456, v5\n\t"
      "v_and_b32 v7, 0x1f0, v5\n\t"
      "v_mfma_f32_32x32x16_bf16 v[176:191], v[72:75], v[16:19], v[176:191]\n\t"
      "ds_read_b64_tr_b16 v[72:73], v3 offset:2048\n\t"
      "ds_read_b64_tr_b16 v[74:75], v3 offset:2304\n\t"
      "v_lshrrev_b32 v40, 7, v5\n\t"
      "v_and_b32 v40, 4, v40\n\t"
      "v_lshl_add_u32 v7, v7, 1, v40\n\t"
      "v_add_f32 v24, v6, v24\n\t"
      "v_add_f32 v25, v6, v25\n\t"
      "v_add_f32 v26, v6, v26\n\t"
      "v_add_f32 v27, v6, v27\n\t"
      "v_add_f32 v28, v6, v28\n\t"
      "v_add_f32 v29, v6, v29\n\t"
      "v_add_f32 v30, v6, v30\n\t"
      "v_add_f32 v31, v6, v31\n\t"
      "v_add_f32 v32, v6, v32\n\t"
      "v_add_f32 v33, v6, v33\n\t"
      "v_add_f32 v34, v6, v34\n\t"
      "v_add_f32 v35, v6, v35\n\t"
      "v_add_f32 v36, v6, v36\n\t"
      "v_add_f32 v37, v6, v37\n\t"
      "v_add_f32 v38, v6, v38\n\t"
      "v_add_f32 v39, v6, v39\n\t"
      "v_mfma_f32_32x32x16_bf16 v[192:207], v[80:83], v[16:19], v[192:207]\n\t"
      "ds_read_b64_tr_b16 v[80:81], v3 offset:4096\n\t"
      "ds_read_b64_tr_b16 v[82:83], v3 offset:4352\n\t"
      "v_exp_f32 v24, v24\n\t"
      "v_exp_f32 v25, v25\n\t"
      "v_mfma_f32_32x32x16_bf16 v[208:223], v[88:91], v[16:19], v[208:223]\n\t"
      "ds_read_b64_tr_b16 v[88:89], v3 offset:6144\n\t"
      "ds_read_b64_tr_b16 v[90:91], v3 offset:6400\n\t"
      "v_exp_f32 v26, v26\n\t"
      "v_exp_f32 v27, v27\n\t"
      "v_mfma_f32_32x32x16_bf16 v[224:239], v[96:99], v[16:19], v[224:239]\n\t"
      "ds_read_b64_tr_b16 v[96:97], v3 offset:8192\n\t"
      "ds_read_b64_tr_b16 v[98:99], v3 offset:8448\n\t"
      "v_exp_f32 v28, v28\n\t"
      "v_exp_f32 v29, v29\n\t"
      "v_mfma_f32_32x32x16_bf16 v[240:255], v[104:107], v[16:19], v[240:255]\n\t"
      "ds_read_b64_tr_b16 v[104:105], v3 offset:10240\n\t"
      "ds_read_b64_tr_b16 v[106:107], v3 offset:10496\n\t"
      "v_exp_f32 v30, v30\n\t"
      "v_exp_f32 v31, v31\n\t"
      "v_mfma_f32_32x32x16_bf16 v[160:175], v[68:71], v[20:23], v[160:175]\n\t"
      "ds_read_b64_tr_b16 v[68:69], v4 offset:0\n\t"
      "ds_read_b64_tr_b16 v[70:71], v4 offset:256\n\t"
      "v_exp_f32 v8, v32\n\t"
      "v_exp_f32 v9, v33\n\t"
      "v_cvt_pk_bf16_f32 v16, v24, v25\n\t"
      "v_mfma_f32_32x32x16_bf16 v[176:191], v[76:79], v[20:23], v[176:191]\n\t"
      "ds_read_b64_tr_b16 v[76:77], v4 offset:2048\n\t"
      "ds_read_b64_tr_b16 v[78:79], v4 offset:2304\n\t"
      "v_exp_f32 v10, v34\n\t"
      "v_exp_f32 v11, v35\n\t"
      "v_cvt_pk_bf16_f32 v17, v26, v27\n\t"
      "v_mfma_f32_32x32x16_bf16 v[192:207], v[84:87], v[20:23], v[192:207]\n\t"
      "ds_read_b64_tr_b16 v[84:85], v4 offset:4096\n\t"
      "ds_read_b64_tr_b16 v[86:87], v4 offset:4352\n\t"
      "v_exp_f32 v12, v36\n\t"
      "v_exp_f32 v13, v37\n\t"
      "v_cvt_pk_bf16_f32 v18, v28, v29\n\t"
      "v_mfma_f32_32x32x16_bf16 v[208:223], v[92:95], v[20:23], v[208:223]\n\t"
      "ds_read_b64_tr_b16 v[92:93], v4 offset:6144\n\t"
      "ds_read_b64_tr_b16 v[94:95], v4 offset:6400\n\t"
      "v_exp_f32 v14, v38\n\t"
      "v_exp_f32 v15, v39\n\t"
      "v_cvt_pk_bf16_f32 v19, v30, v31\n\t"
      "v_mfma_f32_32x32x16_bf16 v[224:239], v[100:103], v[20:23], v[224:239]\n\t"
      "ds_read_b64_tr_b16 v[100:101], v4 offset:8192\n\t"
      "ds_read_b64_tr_b16 v[102:103], v4 offset:8448\n\t"
      "v_add_u32 v7, 162816, v7\n\t"
      "v_add_f32 v0, v0, v24\n\t"
      "v_add_f32 v0, v0, v25\n\t"
      "v_mfma_f32_32x32x16_bf16 v[240:255], v[108:111], v[20:23], v[240:255]\n\t"
      "ds_read_b64_tr_b16 v[108:109], v4 offset:10240\n\t"
      "ds_read_b64_tr_b16 v[110:111], v4 offset:10496\n\t"
      "v_cvt_pk_bf16_f32 v20, v8, v9\n\t"
      "v_cvt_pk_bf16_f32 v21, v10, v11\n\t"
      "v_cvt_pk_bf16_f32 v22, v12, v13\n\t"
      "v_cvt_pk_bf16_f32 v23, v14, v15\n\t"
      "ds_read_b128 v[40:43], v2 offset:12288\n\t"
      "ds_read_b128 v[44:47], v2 offset:13312\n\t"
      "s_branch 31b\n\t"
      "32:\n\t"
      : "+{v[0:7]}"(ra), "+{v[8:15]}"(rb), "+{v[16:19]}"(sel), "+{v[160:191]}"(m0), "+{v[192:223]}"(m1), "+{v[224:231]}"(m2), [cnt] "=&s"(cnt), [dl] "=&s"(dl)
      : [grp] "s"(B_GRP), [msk] "s"(0x24u), [w] "s"(w), [rs] "s"(rs), [lim] "s"(kNormLimit), [tag] "s"(((unsigned long long)tag1(ex) << 32) | tag0(ex))
      : "memory", "scc", "vcc", "v20", "v21", "v22", "v23", "v24", "v25", "v26", "v27", "v28", "v29", "v30", "v31", "v32", "v33", "v34", "v35", "v36", "v37", "v38", "v39", "v40", "v41", "v42", "v43", "v44", "v45", "v46", "v47", "v48", "v49", "v50", "v51", "v52", "v53", "v54", "v55", "v56", "v57", "v58", "v59", "v60", "v61", "v62", "v63", "v64", "v65", "v66", "v67", "v68", "v69", "v70", "v71", "v72", "v73", "v74", "v75", "v76", "v77", "v78", "v79", "v80", "v81", "v82", "v83", "v84", "v85", "v86", "v87", "v88", "v89", "v90", "v91", "v92", "v93", "v94", "v95", "v96", "v97", "v98", "v99", "v100", "v101", "v102", "v103", "v104", "v105", "v106", "v107", "v108", "v109", "v110", "v111", "v112", "v113", "v114", "v115", "v116", "v117", "v118", "v119", "v120", "v121", "v122", "v123", "v124", "v125", "v126", "v127", "v128", "v129", "v130", "v131", "v132", "v133", "v134", "v135", "v136", "v137", "v138", "v139", "v140", "v141", "v142", "v143", "v144", "v145", "v146", "v147", "v148", "v149", "v150", "v151", "v152", "v153", "v154", "v155", "v156", "v157", "v158", "v159", "v232", "v233", "v234", "v235", "v236", "v237", "v238", "v239", "v240", "v241", "v242", "v243", "v244", "v245", "v246", "v247", "v248", "v249", "v250", "v251", "v252", "v253", "v254", "v255");
}

extern "C" __global__ __launch_bounds__(512, 2) void k_fused(const char* a_x, const char* a_wqkv_t, const char* a_wo_t, const char* a_wqkv_s,
                                                             const char* a_wo_s, char* a_ws, char* a_out) {
  __shared__ __attribute__((aligned(1024))) char smem_raw[163840];
  asm volatile("" ::"s"((lptr)smem_raw));
  const int lane = threadIdx.x & 63, w = __builtin_amdgcn_readfirstlane(threadIdx.x >> 6), b = blockIdx.x;
  unsigned xv;
  unsigned long long wsp, outp;
  u32x32 m0, m1; u32x8 m2;
  asm volatile(
      "s_load_dwordx8 s[64:71], %[ka], 0x0\n\t"
      "s_load_dwordx4 s[72:75], %[ka], 0x20\n\t"
      "s_load_dwordx2 s[76:77], %[ka], 0x30\n\t"
      "v_and_b32 v2, 63, %[tid]\n\t"
      "v_lshlrev_b32 v3, 4, v2\n\t"
      "v_mov_b32 v11, 3497984\n\t"
      "s_cmp_lt_u32 %[w], 4\n\t"
      "s_cbranch_scc1 41f\n\t"
      "s_cmp_gt_u32 %[w], 5\n\t"
      "s_cbranch_scc1 43f\n\t"
      "s_cmp_eq_u32 %[w], 5\n\t"
      "s_cbranch_scc1 42f\n\t"
      "s_cmp_ge_u32 %[b], 216\n\t"
      "s_cbranch_scc1 44f\n\t"
      "s_movk_i32 s80, 171\n\t"
      "s_movk_i32 s81, 768\n\t"
      "v_mov_b32 v4, v2\n\t"
      "v_or_b32 v5, 64, v2\n\t"
      "v_or_b32 v6, 0x80, v2\n\t"
      "v_mul_u32_u24 v4, s80, v4\n\t"
      "v_mul_u32_u24 v5, s80, v5\n\t"
      "v_mul_u32_u24 v6, s80, v6\n\t"
      "v_lshrrev_b32 v4, 12, v4\n\t"
      "v_lshrrev_b32 v5, 12, v5\n\t"
      "v_lshrrev_b32 v6, 12, v6\n\t"
      "v_mad_u32_u24 v4, v4, s81, v3\n\t"
      "v_mad_u32_u24 v5, v5, s81, v3\n\t"
      "v_mad_u32_u24 v6, v6, s81, v3\n\t"
      "s_cmp_ge_u32 %[b], 72\n\t"
      "s_cselect_b32 s82, 72, 0\n\t"
      "s_cmp_ge_u32 %[b], 0x90\n\t"
      "s_cselect_b32 s82, 0x90, s82\n\t"
      "s_mov_b32 s58, s82\n\t"
      "s_sub_u32 s82, %[b], s82\n\t"
      "s_mul_i32 s90, s82, 43\n\t"
      "s_lshr_b32 s90, s90, 8\n\t"
      "s_mul_i32 s91, s90, 6\n\t"
      "s_sub_u32 s91, s82, s91\n\t"
      "s_mul_i32 s83, s90, 6\n\t"
      "s_add_u32 s58, s58, s83\n\t"
      "s_add_u32 s58, s58, s91\n\t"
      "s_mul_i32 s83, s90, 0x4800\n\t"
      "s_cmp_ge_u32 %[b], 0x90\n\t"
      "s_cbranch_scc1 45f\n\t"
      "v_lshrrev_b32 v7, 2, v2\n\t"
      "v_and_b32 v8, 48, v3\n\t"
      "s_movk_i32 s80, 384\n\t"
      "v_mad_u32_u24 v7, v7, s80, v8\n\t"
      "v_add_u32 v8, 0x1400, v7\n\t"
      "v_add_u32 v9, 0x2800, v7\n\t"
      "s_addk_i32 s83, 768\n\t"
      "s_lshl_b32 s84, s91, 6\n\t"
      "s_movk_i32 s85, 0x4800\n\t"
      "s_cmp_ge_u32 %[b], 72\n\t"
      "s_waitcnt lgkmcnt(0)\n\t"
      "s_cselect_b64 s[86:87], s[70:71], s[66:67]\n\t"
      "s_cselect_b64 s[88:89], s[72:73], s[68:69]\n\t"
      "s_branch 46f\n\t"
      "45:\n\t"
      "v_mov_b32 v7, v4\n\t"
      "v_mov_b32 v8, v5\n\t"
      "v_mov_b32 v9, v6\n\t"
      "s_mul_i32 s84, s91, 0x9000\n\t"
      "s_addk_i32 s84, 384\n\t"
      "s_movk_i32 s85, 0x2400\n\t"
      "s_waitcnt lgkmcnt(0)\n\t"
      "s_mov_b64 s[86:87], s[70:71]\n\t"
      "s_mov_b64 s[88:89], s[70:71]\n\t"
      "46:\n\t"
      "s_add_u32 s88, s88, s84\n\t"
      "s_addc_u32 s89, s89, 0\n\t"
      "s_add_u32 s86, s86, s83\n\t"
      "s_addc_u32 s87, s87, 0\n\t"
      "s_mov_b32 s82, 116736\n\t"
      "47:\n\t"
      "s_mov_b32 m0, s82\n\t"
      "s_addk_i32 s82, 0xc00\n\t"
      "global_load_lds_dwordx4 v7, s[88:89]\n\t"
      "global_load_lds_dwordx4 v8, s[88:89] offset:1024\n\t"
      "global_load_lds_dwordx4 v9, s[88:89] offset:2048\n\t"
      "s_add_u32 s88, s88, s85\n\t"
      "s_addc_u32 s89, s89, 0\n\t"
      "s_cmp_lg_u32 s82, 129024\n\t"
      "s_cbranch_scc1 47b\n\t"
      "s_mov_b32 m0, 113664\n\t"
      "s_add_u32 s88, s86, 0x2400\n\t"
      "s_addc_u32 s89, s87, 0\n\t"
      "s_add_u32 s86, s86, 0xc00\n\t"
      "s_addc_u32 s87, s87, 0\n\t"
      "global_load_lds_dwordx4 v4, s[86:87] offset:-3072\n\t"
      "global_load_lds_dwordx4 v5, s[86:87] offset:-2048\n\t"
      "global_load_lds_dwordx4 v6, s[86:87] offset:-1024\n\t"
      "global_load_lds_dwordx4 v4, s[88:89]\n\t"
      "global_load_lds_dwordx4 v5, s[88:89] offset:1024\n\t"
      "global_load_lds_dwordx4 v6, s[88:89] offset:2048\n\t"
      "s_branch 44f\n\t"
      "43:\n\t"
      "s_movk_i32 s80, 171\n\t"
      "s_movk_i32 s81, 0x180\n\t"
      "v_mov_b32 v20, v2\n\t"
      "v_or_b32 v21, 64, v2\n\t"
      "v_or_b32 v22, 0x80, v2\n\t"
      "v_mul_u32_u24 v7, s80, v20\n\t"
      "v_mul_u32_u24 v8, s80, v21\n\t"
      "v_mul_u32_u24 v9, s80, v22\n\t"
      "v_lshrrev_b32 v7, 13, v7\n\t"
      "v_lshlrev_b32 v10, 2, v7\n\t"
      "v_xor_b32 v20, v20, v10\n\t"
      "v_mul_u32_u24 v10, s80, v20\n\t"
      "v_lshrrev_b32 v10, 11, v10\n\t"
      "v_and_b32 v10, 2, v10\n\t"
      "v_xor_b32 v20, v20, v10\n\t"
      "v_lshlrev_b32 v20, 4, v20\n\t"
      "v_mad_u32_u24 v20, v7, s81, v20\n\t"
      "v_lshrrev_b32 v8, 13, v8\n\t"
      "v_lshlrev_b32 v10, 2, v8\n\t"
      "v_xor_b32 v21, v21, v10\n\t"
      "v_mul_u32_u24 v10, s80, v21\n\t"
      "v_lshrrev_b32 v10, 11, v10\n\t"
      "v_and_b32 v10, 2, v10\n\t"
      "v_xor_b32 v21, v21, v10\n\t"
      "v_lshlrev_b32 v21, 4, v21\n\t"
      "v_mad_u32_u24 v21, v8, s81, v21\n\t"
      "v_lshrrev_b32 v9, 13, v9\n\t"
      "v_lshlrev_b32 v10, 2, v9\n\t"
      "v_xor_b32 v22, v22, v10\n\t"
      "v_mul_u32_u24 v10, s80, v22\n\t"
      "v_lshrrev_b32 v10, 11, v10\n\t"
      "v_and_b32 v10, 2, v10\n\t"
      "v_xor_b32 v22, v22, v10\n\t"
      "v_lshlrev_b32 v22, 4, v22\n\t"
      "v_mad_u32_u24 v22, v9, s81, v22\n\t"
      "v_add_u32 v20, 0x800, v20\n\t"
      "v_add_u32 v21, 0x400, v21\n\t"
      "s_and_b32 s83, %[w], 1\n\t"
      "s_mul_i32 s84, s83, 0x4800\n\t"
      "s_mul_i32 s83, s83, 0x6c00\n\t"
      "s_addk_i32 s83, 0xf800\n\t"
      "s_lshr_b32 s85, %[b], 3\n\t"
      "s_add_u32 s82, s84, 15360\n\t"
      "s_waitcnt lgkmcnt(0)\n\t"
      "global_load_dword v32, v11, s[74:75] sc1\n\t"
      "s_ashr_i32 s80, s83, 31\n\t"
      "s_add_u32 s86, s66, s83\n\t"
      "s_addc_u32 s87, s67, s80\n\t"
      "s_sleep 6\n\t"
      "50:\n\t"
      "s_and_b32 s80, s85, 3\n\t"
      "s_mul_i32 s80, s80, 0xd800\n\t"
      "s_add_u32 s88, s86, s80\n\t"
      "s_addc_u32 s89, s87, 0\n\t"
      "s_add_u32 s83, s84, 0x4800\n\t"
      "s_add_u32 s85, s85, 1\n\t"
      "51:\n\t"
      "s_mov_b32 m0, s84\n\t"
      "s_addk_i32 s84, 0xc00\n\t"
      "global_load_lds_dwordx4 v20, s[88:89]\n\t"
      "global_load_lds_dwordx4 v21, s[88:89] offset:1024\n\t"
      "global_load_lds_dwordx4 v22, s[88:89] offset:2048\n\t"
      "s_add_u32 s88, s88, 0x1200\n\t"
      "s_addc_u32 s89, s89, 0\n\t"
      "s_cmp_eq_u32 s84, s82\n\t"
      "s_cbranch_scc1 56f\n\t"
      "55:\n\t"
      "s_cmp_lg_u32 s84, s83\n\t"
      "s_cbranch_scc1 51b\n\t"
      "s_addk_i32 s84, 0x4800\n\t"
      "s_cmp_lt_u32 s84, 73728\n\t"
      "s_cbranch_scc1 50b\n\t"
      "s_cmp_lt_u32 s84, 147456\n\t"
      "s_cbranch_scc0 52f\n\t"
      "s_waitcnt vmcnt(18)\n\t"
      "s_barrier\n\t"
      "s_branch 50b\n\t"
      "41:\n\t"
      "v_lshrrev_b32 v4, 3, v2\n\t"
      "s_lshl_b32 s80, %[b], 2\n\t"
      "s_add_u32 s80, s80, %[w]\n\t"
      "s_mul_i32 s80, s80, 384\n\t"
      "s_mov_b32 s81, 0x60000\n\t"
      "v_mul_u32_u24 v4, s81, v4\n\t"
      "v_and_b32 v5, 0x70, v3\n\t"
      "v_add3_u32 v4, v4, v5, s80\n\t"
      "s_waitcnt lgkmcnt(0)\n\t"
      "s_sleep 8\n\t"
      "global_load_dwordx4 v[20:23], v4, s[64:65] nt\n\t"
      "global_load_dwordx4 v[24:27], v4, s[64:65] offset:128 nt\n\t"
      "global_load_dwordx4 v[28:31], v4, s[64:65] offset:256 nt\n\t"
      "global_load_dword v32, v11, s[74:75] sc1\n\t"
      "s_branch 70f\n\t"
      "42:\n\t"
      "s_lshr_b32 s80, %[b], 3\n\t"
      "v_lshl_add_u32 v4, v2, 5, s80\n\t"
      "v_lshlrev_b32 v4, 7, v4\n\t"
      "v_min_u32 v4, .Lkf_last-.Lkf_pc-4, v4\n\t"
      "s_getpc_b64 s[80:81]\n\t"
      ".Lkf_pc:\n\t"
      "s_mov_b32 m0, 0x27c00\n\t"
      "s_mov_b64 exec, 0xff\n\t"
      "global_load_lds_dword v4, s[80:81]\n\t"
      "s_mov_b64 exec, -1\n\t"
      "44:\n\t"
      "s_waitcnt lgkmcnt(0)\n\t"
      "global_load_dword v32, v11, s[74:75] sc1\n\t"
      "56:\n\t"
      "s_and_b32 s60, %[w], 3\n\t"
      "v_and_b32 v5, 15, v2\n\t"
      "v_lshrrev_b32 v6, 4, v2\n\t"
      "s_movk_i32 s80, 528\n\t"
      "v_mul_u32_u24 v7, s80, v6\n\t"
      "v_lshl_add_u32 v8, v5, 4, v7\n\t"
      "v_add_u32 v12, 147456, v8\n\t"
      "v_and_b32 v8, 3, v2\n\t"
      "v_bfe_u32 v9, v2, 2, 2\n\t"
      "v_lshl_add_u32 v10, v8, 3, v9\n\t"
      "s_mul_i32 s81, s60, 3168\n\t"
      "s_add_u32 s81, s81, 36864\n\t"
      "v_lshl_add_u32 v7, v10, 4, v7\n\t"
      "v_add_u32 v14, s81, v7\n\t"
      "v_lshrrev_b32 v4, 5, v2\n\t"
      "v_mul_u32_u24 v7, s80, v4\n\t"
      "v_lshl_add_u32 v7, v10, 4, v7\n\t"
      "v_and_b32 v11, 1, v6\n\t"
      "v_lshl_add_u32 v7, v11, 3, v7\n\t"
      "s_addk_i32 s81, 2112\n\t"
      "v_add_u32 v15, s81, v7\n\t"
      "v_xor_b32 v7, v4, v2\n\t"
      "v_and_b32 v7, 1, v7\n\t"
      "v_lshl_add_u32 v7, v7, 2, v10\n\t"
      "v_lshl_add_u32 v7, v11, 5, v7\n\t"
      "v_lshlrev_b32 v13, 4, v7\n\t"
      "v_lshlrev_b32 v7, 5, v5\n\t"
      "v_add_u32 v17, 160128, v7\n\t"
      "s_lshl_b32 s81, s60, 3\n\t"
      "v_lshl_add_u32 v7, v4, 9, v17\n\t"
      "v_lshl_add_u32 v7, v11, 2, v7\n\t"
      "v_add_u32 v16, s81, v7\n\t"
      "v_lshl_add_u32 v7, v11, 2, v10\n\t"
      "s_lshl_b32 s81, %[b], 8\n\t"
      "s_add_u32 s81, s81, 3366912\n\t"
      "v_lshl_add_u32 v34, v7, 3, s81\n\t"
      "s_mov_b32 s80, 0x60000\n\t"
      "v_mul_u32_u24 v9, s80, v9\n\t"
      "v_lshl_add_u32 v9, v8, 5, v9\n\t"
      "s_lshr_b32 s80, %[b], 3\n\t"
      "s_mul_i32 s80, s80, 0x3000\n\t"
      "s_mul_i32 s81, s60, 0xc00\n\t"
      "s_add_u32 s80, s80, s81\n\t"
      "s_xor_b32 s81, %[b], s60\n\t"
      "s_and_b32 s81, s81, 1\n\t"
      "s_and_b32 s61, %[b], 6\n\t"
      "s_or_b32 s81, s81, s61\n\t"
      "s_lshl_b32 s81, s81, 7\n\t"
      "s_add_u32 s80, s80, s81\n\t"
      "v_add_u32 v9, s80, v9\n\t"
      "s_bfe_u32 s80, %[b], 0x10002\n\t"
      "v_xor_b32 v7, s80, v11\n\t"
      "v_lshl_add_u32 v7, v7, 4, v9\n\t"
      "v_lshlrev_b32 v8, 7, v4\n\t"
      "v_xor_b32 v7, v7, v8\n\t"
      "v_lshl_add_u32 v18, v4, 10, v7\n\t"
      "v_xor_b32 v7, s80, v4\n\t"
      "v_lshl_add_u32 v7, v7, 4, v9\n\t"
      "s_mov_b32 s80, 0x180000\n\t"
      "v_mul_u32_u24 v8, s80, v11\n\t"
      "v_add_u32 v19, v7, v8\n\t"
      "s_add_u32 s62, s74, 0x180000\n\t"
      "s_addc_u32 s63, s75, 0\n\t"
      "v_min_u32 v7, 23, v2\n\t"
      "v_lshrrev_b32 v8, 1, v7\n\t"
      "v_and_b32 v7, 1, v7\n\t"
      "v_mad_u32_u24 v7, v8, 6, v7\n\t"
      "s_mul_i32 s80, s60, 3\n\t"
      "s_lshr_b32 s80, s80, 1\n\t"
      "s_lshl_b32 s80, s80, 7\n\t"
      "s_add_u32 s80, s80, 3465216\n\t"
      "v_lshl_add_u32 v33, v7, 7, s80\n\t"
      "s_mul_i32 s80, s60, 0x4800\n\t"
      "s_add_u32 s80, s80, 3148800\n\t"
      "s_add_u32 s68, s74, s80\n\t"
      "s_addc_u32 s69, s75, 0\n\t"
      "s_add_u32 s70, s68, 0x1800\n\t"
      "s_addc_u32 s71, s69, 0\n\t"
      "s_add_u32 s72, s68, 0x3000\n\t"
      "s_addc_u32 s73, s69, 0\n\t"
      "s_mov_b32 s94, 0xffff0000\n\t"
      "s_cmp_lt_u32 %[w], 6\n\t"
      "s_cbranch_scc1 58f\n\t"
      "s_waitcnt vmcnt(6)\n\t"
      "v_readfirstlane_b32 s96, v32\n\t"
      "s_nop 0\n\t"
      "s_mul_i32 s98, s96, 0x9E3779B1\n\t"
      "s_add_u32 s98, s98, 0x7F4A7C15\n\t"
      "s_xor_b32 s99, s98, 0x5bd1e995\n\t"
      "s_sleep 1\n\t"
      "s_branch 55b\n\t"
      "58:\n\t"
      "s_cmp_eq_u32 %[w], 5\n\t"
      "s_cbranch_scc1 71f\n\t"
      "s_cmp_ge_u32 %[b], 216\n\t"
      "s_cbranch_scc1 71f\n\t"
      "v_and_b32 v5, 31, v2\n\t"
      "v_mov_b32 v7, v2\n\t"
      "s_mov_b32 s84, 1.0\n\t"
      "s_and_b32 s81, s90, 1\n\t"
      "s_lshl_b32 s81, s81, 9\n\t"
      "s_lshr_b32 s82, s90, 1\n\t"
      "s_lshl_b32 s82, s82, 10\n\t"
      "s_add_u32 s82, s82, s81\n\t"
      "s_sub_u32 s80, %[b], 72\n\t"
      "s_cmp_ge_u32 s80, 72\n\t"
      "s_cbranch_scc1 73f\n\t"
      "v_and_b32 v7, 51, v2\n\t"
      "v_and_b32 v6, 4, v2\n\t"
      "v_lshl_or_b32 v7, v6, 1, v7\n\t"
      "v_and_b32 v6, 8, v2\n\t"
      "v_lshrrev_b32 v6, 1, v6\n\t"
      "v_or_b32 v7, v7, v6\n\t"
      "s_lshl_b32 s80, s91, 1\n\t"
      "v_bfe_u32 v8, v2, 2, 1\n\t"
      "v_add_u32 v8, s80, v8\n\t"
      "v_mov_b32 v6, v5\n\t"
      "s_add_u32 s82, s82, 3293184\n\t"
      "s_mov_b64 vcc, -1\n\t"
      "s_branch 68f\n\t"
      "73:\n\t"
      "s_lshl_b32 s80, s91, 5\n\t"
      "v_add_u32 v6, s80, v5\n\t"
      "s_movk_i32 s80, 171\n\t"
      "v_mul_u32_u24 v8, s80, v6\n\t"
      "v_lshrrev_b32 v8, 13, v8\n\t"
      "v_mul_u32_u24 v9, 48, v8\n\t"
      "v_sub_u32 v6, v6, v9\n\t"
      "v_cmp_gt_u32 vcc, 32, v6\n\t"
      "v_bfe_u32 v9, v6, 2, 1\n\t"
      "v_mul_u32_u24 v8, 3, v8\n\t"
      "v_cndmask_b32 v9, 2, v9, vcc\n\t"
      "v_add_u32 v8, v8, v9\n\t"
      "s_cmp_ge_u32 %[b], 72\n\t"
      "s_cselect_b32 s80, 73728, 0\n\t"
      "s_cselect_b32 s84, 0x3dd53b95, s84\n\t"
      "s_add_u32 s82, s82, s80\n\t"
      "s_add_u32 s82, s82, 3145728\n\t"
      "68:\n\t"
      "v_lshrrev_b32 v9, 3, v6\n\t"
      "v_and_b32 v10, 3, v6\n\t"
      "v_lshl_add_u32 v9, v9, 2, v10\n\t"
      "v_subrev_u32 v6, 32, v6\n\t"
      "v_cndmask_b32 v6, v6, v9, vcc\n\t"
      "v_and_b32 v9, 32, v2\n\t"
      "v_lshrrev_b32 v9, 1, v9\n\t"
      "v_add_u32 v6, v6, v9\n\t"
      "v_lshlrev_b32 v6, 4, v6\n\t"
      "s_movk_i32 s81, 0x1800\n\t"
      "v_mad_u32_u24 v6, v8, s81, v6\n\t"
      "v_add_u32 v10, s82, v6\n\t"
      "v_and_b32 v7, 15, v7\n\t"
      "s_movk_i32 s80, 384\n\t"
      "v_and_b32 v6, 48, v2\n\t"
      "v_mad_u32_u24 v7, v7, s80, v6\n\t"
      "v_add_u32 v7, 110592, v7\n\t"
      "v_lshlrev_b32 v6, 5, v6\n\t"
      "v_and_b32 v8, 12, v2\n\t"
      "v_lshl_add_u32 v6, v8, 4, v6\n\t"
      "v_and_b32 v8, 3, v2\n\t"
      "v_lshl_add_u32 v6, v8, 3, v6\n\t"
      "v_add_u32 v6, 116736, v6\n\t"
      "s_lshl_b32 s80, s58, 7\n\t"
      "s_add_u32 s80, s80, 3465216\n\t"
      "v_add_u32 v9, s80, v3\n\t"
      "s_cmp_ge_u32 %[b], 0x90\n\t"
      "s_waitcnt vmcnt(0)\n\t"
      "s_cbranch_scc1 75f\n\t"
      "ds_read_b128 v[108:111], v7 offset:0\n\t"
      "ds_read_b64_tr_b16 v[132:133], v6 offset:0\n\t"
      "ds_read_b64_tr_b16 v[134:135], v6 offset:256\n\t"
      "ds_read_b64_tr_b16 v[156:157], v6 offset:32\n\t"
      "ds_read_b64_tr_b16 v[158:159], v6 offset:288\n\t"
      "ds_read_b128 v[112:115], v7 offset:64\n\t"
      "ds_read_b64_tr_b16 v[136:137], v6 offset:2048\n\t"
      "ds_read_b64_tr_b16 v[138:139], v6 offset:2304\n\t"
      "ds_read_b64_tr_b16 v[160:161], v6 offset:2080\n\t"
      "ds_read_b64_tr_b16 v[162:163], v6 offset:2336\n\t"
      "ds_read_b128 v[116:119], v7 offset:128\n\t"
      "ds_read_b64_tr_b16 v[140:141], v6 offset:4096\n\t"
      "ds_read_b64_tr_b16 v[142:143], v6 offset:4352\n\t"
      "ds_read_b64_tr_b16 v[164:165], v6 offset:4128\n\t"
      "ds_read_b64_tr_b16 v[166:167], v6 offset:4384\n\t"
      "ds_read_b128 v[120:123], v7 offset:192\n\t"
      "ds_read_b64_tr_b16 v[144:145], v6 offset:6144\n\t"
      "ds_read_b64_tr_b16 v[146:147], v6 offset:6400\n\t"
      "ds_read_b64_tr_b16 v[168:169], v6 offset:6176\n\t"
      "ds_read_b64_tr_b16 v[170:171], v6 offset:6432\n\t"
      "ds_read_b128 v[124:127], v7 offset:256\n\t"
      "ds_read_b64_tr_b16 v[148:149], v6 offset:8192\n\t"
      "ds_read_b64_tr_b16 v[150:151], v6 offset:8448\n\t"
      "ds_read_b64_tr_b16 v[172:173], v6 offset:8224\n\t"
      "ds_read_b64_tr_b16 v[174:175], v6 offset:8480\n\t"
      "ds_read_b128 v[128:131], v7 offset:320\n\t"
      "ds_read_b64_tr_b16 v[152:153], v6 offset:10240\n\t"
      "ds_read_b64_tr_b16 v[154:155], v6 offset:10496\n\t"
      "ds_read_b64_tr_b16 v[176:177], v6 offset:10272\n\t"
      "ds_read_b64_tr_b16 v[178:179], v6 offset:10528\n\t"
      "s_branch 76f\n\t"
      "75:\n\t"
      "ds_read_b128 v[108:111], v7 offset:0\n\t"
      "ds_read_b128 v[132:135], v7 offset:6144\n\t"
      "ds_read_b128 v[156:159], v7 offset:12288\n\t"
      "ds_read_b128 v[112:115], v7 offset:64\n\t"
      "ds_read_b128 v[136:139], v7 offset:6208\n\t"
      "ds_read_b128 v[160:163], v7 offset:12352\n\t"
      "ds_read_b128 v[116:119], v7 offset:128\n\t"
      "ds_read_b128 v[140:143], v7 offset:6272\n\t"
      "ds_read_b128 v[164:167], v7 offset:12416\n\t"
      "ds_read_b128 v[120:123], v7 offset:192\n\t"
      "ds_read_b128 v[144:147], v7 offset:6336\n\t"
      "ds_read_b128 v[168:171], v7 offset:12480\n\t"
      "ds_read_b128 v[124:127], v7 offset:256\n\t"
      "ds_read_b128 v[148:151], v7 offset:6400\n\t"
      "ds_read_b128 v[172:175], v7 offset:12544\n\t"
      "ds_read_b128 v[128:131], v7 offset:320\n\t"
      "ds_read_b128 v[152:155], v7 offset:6464\n\t"
      "ds_read_b128 v[176:179], v7 offset:12608\n\t"
      "s_waitcnt lgkmcnt(0)\n\t"
      "76:\n\t"
      "v_readfirstlane_b32 s96, v32\n\t"
      "s_waitcnt lgkmcnt(15)\n\t"
      "v_mfma_f32_16x16x32_bf16 v[180:183], v[108:111], v[132:135], 0\n\t"
      "v_mfma_f32_16x16x32_bf16 v[184:187], v[108:111], v[156:159], 0\n\t"
      "s_waitcnt lgkmcnt(15)\n\t"
      "v_mfma_f32_16x16x32_bf16 v[180:183], v[112:115], v[136:139], v[180:183]\n\t"
      "v_mfma_f32_16x16x32_bf16 v[184:187], v[112:115], v[160:163], v[184:187]\n\t"
      "s_waitcnt lgkmcnt(15)\n\t"
      "v_mfma_f32_16x16x32_bf16 v[180:183], v[116:119], v[140:143], v[180:183]\n\t"
      "v_mfma_f32_16x16x32_bf16 v[184:187], v[116:119], v[164:167], v[184:187]\n\t"
      "s_waitcnt lgkmcnt(10)\n\t"
      "v_mfma_f32_16x16x32_bf16 v[180:183], v[120:123], v[144:147], v[180:183]\n\t"
      "v_mfma_f32_16x16x32_bf16 v[184:187], v[120:123], v[168:171], v[184:187]\n\t"
      "s_waitcnt lgkmcnt(5)\n\t"
      "v_mfma_f32_16x16x32_bf16 v[180:183], v[124:127], v[148:151], v[180:183]\n\t"
      "v_mfma_f32_16x16x32_bf16 v[184:187], v[124:127], v[172:175], v[184:187]\n\t"
      "s_waitcnt lgkmcnt(0)\n\t"
      "v_mfma_f32_16x16x32_bf16 v[180:183], v[128:131], v[152:155], v[180:183]\n\t"
      "v_mfma_f32_16x16x32_bf16 v[184:187], v[128:131], v[176:179], v[184:187]\n\t"
      "s_mul_i32 s98, s96, 0x9E3779B1\n\t"
      "s_add_u32 s98, s98, 0x7F4A7C15\n\t"
      "s_xor_b32 s99, s98, 0x5bd1e995\n\t"
      "v_mov_b32 v108, s98\n\t"
      "v_mov_b32 v109, s99\n\t"
      "v_mov_b32 v110, s98\n\t"
      "v_mov_b32 v111, s99\n\t"
      "s_nop 0\n\t"
      "v_mul_f32 v180, s84, v180\n\t"
      "v_mul_f32 v181, s84, v181\n\t"
      "v_mul_f32 v182, s84, v182\n\t"
      "v_mul_f32 v183, s84, v183\n\t"
      "v_mul_f32 v184, s84, v184\n\t"
      "v_mul_f32 v185, s84, v185\n\t"
      "v_mul_f32 v186, s84, v186\n\t"
      "v_mul_f32 v187, s84, v187\n\t"
      "v_cvt_pk_bf16_f32 v112, v180, v181\n\t"
      "v_cvt_pk_bf16_f32 v114, v184, v185\n\t"
      "v_cvt_pk_bf16_f32 v113, v182, v183\n\t"
      "v_cvt_pk_bf16_f32 v115, v186, v187\n\t"
      "v_permlane16_swap_b32 v112, v114\n\t"
      "s_nop 0\n\t"
      "v_permlane16_swap_b32 v113, v115\n\t"
      "global_store_dwordx4 v10, v[112:115], s[74:75] sc1\n\t"
      "s_waitcnt vmcnt(0)\n\t"
      "s_mov_b64 exec, 0xff\n\t"
      "global_store_dwordx4 v9, v[108:111], s[74:75] sc1\n\t"
      "s_mov_b64 exec, -1\n\t"
      "s_branch 77f\n\t"
      "71:\n\t"
      "s_waitcnt vmcnt(0)\n\t"
      "v_readfirstlane_b32 s96, v32\n\t"
      "s_nop 0\n\t"
      "s_mul_i32 s98, s96, 0x9E3779B1\n\t"
      "s_add_u32 s98, s98, 0x7F4A7C15\n\t"
      "s_xor_b32 s99, s98, 0x5bd1e995\n\t"
      "77:\n\t"
      "s_barrier\n\t"
      "37:\n\t"
      "s_barrier\n\t"
      "s_sleep 6\n\t"
      "global_load_dwordx2 v[38:39], v33, s[74:75] sc1\n\t"
      "ds_read_b128 v[112:115], v12 offset:0\n\t"
      "ds_read_b128 v[116:119], v12 offset:256\n\t"
      "ds_read_b128 v[120:123], v12 offset:2112\n\t"
      "ds_read_b128 v[124:127], v12 offset:2368\n\t"
      "ds_read_b128 v[128:131], v12 offset:4224\n\t"
      "ds_read_b128 v[132:135], v12 offset:4480\n\t"
      "ds_read_b128 v[136:139], v12 offset:6336\n\t"
      "ds_read_b128 v[140:143], v12 offset:6592\n\t"
      "ds_read_b128 v[144:147], v12 offset:8448\n\t"
      "ds_read_b128 v[148:151], v12 offset:8704\n\t"
      "ds_read_b128 v[152:155], v12 offset:10560\n\t"
      "ds_read_b128 v[156:159], v12 offset:10816\n\t"
      "s_barrier\n\t"
      "s_waitcnt vmcnt(0)\n\t"
      "v_cmp_ne_u64 vcc, s[98:99], v[38:39]\n\t"
      "s_cbranch_vccnz 78f\n\t"
      "global_load_dwordx4 v[40:43], v3, s[68:69] offset:-3072 sc1\n\t"
      "global_load_dwordx4 v[44:47], v3, s[70:71] offset:-3072 sc1\n\t"
      "global_load_dwordx4 v[48:51], v3, s[72:73] offset:-3072 sc1\n\t"
      "global_load_dwordx4 v[52:55], v3, s[68:69] offset:-2048 sc1\n\t"
      "global_load_dwordx4 v[56:59], v3, s[70:71] offset:-2048 sc1\n\t"
      "global_load_dwordx4 v[60:63], v3, s[72:73] offset:-2048 sc1\n\t"
      "s_barrier\n\t"
      "s_branch 84f\n\t"
      "52:\n\t"
      "s_waitcnt vmcnt(18)\n\t"
      "global_load_dwordx2 v[38:39], v33, s[74:75] sc1\n\t"
      "s_barrier\n\t"
      "ds_read_b128 v[112:115], v12 offset:0\n\t"
      "ds_read_b128 v[116:119], v12 offset:256\n\t"
      "ds_read_b128 v[120:123], v12 offset:2112\n\t"
      "ds_read_b128 v[124:127], v12 offset:2368\n\t"
      "ds_read_b128 v[128:131], v12 offset:4224\n\t"
      "ds_read_b128 v[132:135], v12 offset:4480\n\t"
      "ds_read_b128 v[136:139], v12 offset:6336\n\t"
      "ds_read_b128 v[140:143], v12 offset:6592\n\t"
      "ds_read_b128 v[144:147], v12 offset:8448\n\t"
      "ds_read_b128 v[148:151], v12 offset:8704\n\t"
      "ds_read_b128 v[152:155], v12 offset:10560\n\t"
      "ds_read_b128 v[156:159], v12 offset:10816\n\t"
      "s_waitcnt vmcnt(1)\n\t"
      "s_branch 79f\n\t"
      "78:\n\t"
      "global_load_dwordx2 v[38:39], v33, s[74:75] sc1\n\t"
      "79:\n\t"
      "s_barrier\n\t"
      "s_mov_b32 s97, 0x10000\n\t"
      "82:\n\t"
      "s_waitcnt vmcnt(0)\n\t"
      "v_cmp_ne_u64 vcc, s[98:99], v[38:39]\n\t"
      "s_cbranch_vccz 83f\n\t"
      "s_sleep 1\n\t"
      "global_load_dwordx2 v[38:39], v33, s[74:75] sc1\n\t"
      "s_sub_u32 s97, s97, 1\n\t"
      "s_cbranch_scc0 82b\n\t"
      "s_waitcnt vmcnt(0)\n\t"
      "83:\n\t"
      "global_load_dwordx4 v[40:43], v3, s[68:69] offset:-3072 sc1\n\t"
      "global_load_dwordx4 v[44:47], v3, s[70:71] offset:-3072 sc1\n\t"
      "global_load_dwordx4 v[48:51], v3, s[72:73] offset:-3072 sc1\n\t"
      "global_load_dwordx4 v[52:55], v3, s[68:69] offset:-2048 sc1\n\t"
      "global_load_dwordx4 v[56:59], v3, s[70:71] offset:-2048 sc1\n\t"
      "global_load_dwordx4 v[60:63], v3, s[72:73] offset:-2048 sc1\n\t"
      "84:\n\t"
      "global_load_dwordx4 v[64:67], v3, s[68:69] offset:-1024 sc1\n\t"
      "global_load_dwordx4 v[68:71], v3, s[70:71] offset:-1024 sc1\n\t"
      "global_load_dwordx4 v[72:75], v3, s[72:73] offset:-1024 sc1\n\t"
      "global_load_dwordx4 v[76:79], v3, s[68:69] offset:0 sc1\n\t"
      "global_load_dwordx4 v[80:83], v3, s[70:71] offset:0 sc1\n\t"
      "global_load_dwordx4 v[84:87], v3, s[72:73] offset:0 sc1\n\t"
      "s_waitcnt lgkmcnt(0)\n\t"
      "global_load_dwordx4 v[88:91], v3, s[68:69] offset:1024 sc1\n\t"
      "s_waitcnt vmcnt(12)\n\t"
      "v_mfma_f32_16x16x32_bf16 v[160:163], v[112:115], v[40:43], 0\n\t"
      "v_mfma_f32_16x16x32_bf16 v[164:167], v[116:119], v[40:43], 0\n\t"
      "global_load_dwordx4 v[92:95], v3, s[70:71] offset:1024 sc1\n\t"
      "s_waitcnt vmcnt(12)\n\t"
      "v_mfma_f32_16x16x32_bf16 v[168:171], v[112:115], v[44:47], 0\n\t"
      "v_mfma_f32_16x16x32_bf16 v[172:175], v[116:119], v[44:47], 0\n\t"
      "global_load_dwordx4 v[96:99], v3, s[72:73] offset:1024 sc1\n\t"
      "s_waitcnt vmcnt(12)\n\t"
      "v_mfma_f32_16x16x32_bf16 v[176:179], v[112:115], v[48:51], 0\n\t"
      "v_mfma_f32_16x16x32_bf16 v[180:183], v[116:119], v[48:51], 0\n\t"
      "global_load_dwordx4 v[100:103], v3, s[68:69] offset:2048 sc1\n\t"
      "s_waitcnt vmcnt(12)\n\t"
      "v_mfma_f32_16x16x32_bf16 v[160:163], v[120:123], v[52:55], v[160:163]\n\t"
      "v_mfma_f32_16x16x32_bf16 v[164:167], v[124:127], v[52:55], v[164:167]\n\t"
      "global_load_dwordx4 v[104:107], v3, s[70:71] offset:2048 sc1\n\t"
      "s_waitcnt vmcnt(12)\n\t"
      "v_mfma_f32_16x16x32_bf16 v[168:171], v[120:123], v[56:59], v[168:171]\n\t"
      "v_mfma_f32_16x16x32_bf16 v[172:175], v[124:127], v[56:59], v[172:175]\n\t"
      "global_load_dwordx4 v[108:111], v3, s[72:73] offset:2048 sc1\n\t"
      "s_waitcnt vmcnt(12)\n\t"
      "v_mfma_f32_16x16x32_bf16 v[176:179], v[120:123], v[60:63], v[176:179]\n\t"
      "v_mfma_f32_16x16x32_bf16 v[180:183], v[124:127], v[60:63], v[180:183]\n\t"
      "s_barrier\n\t"
      "ds_read_b128 v[208:211], v13 offset:0\n\t"
      "ds_read_b128 v[212:215], v13 offset:1024\n\t"
      "ds_read_b128 v[216:219], v13 offset:2048\n\t"
      "ds_read_b128 v[220:223], v13 offset:3072\n\t"
      "ds_read_b128 v[224:227], v14 offset:0\n\t"
      "ds_read_b64 v[228:229], v15 offset:0\n\t"
      "ds_read_b128 v[230:233], v14 offset:64\n\t"
      "ds_read_b64 v[234:235], v15 offset:64\n\t"
      "s_waitcnt vmcnt(11)\n\t"
      "v_mfma_f32_16x16x32_bf16 v[160:163], v[128:131], v[64:67], v[160:163]\n\t"
      "v_mfma_f32_16x16x32_bf16 v[164:167], v[132:135], v[64:67], v[164:167]\n\t"
      "s_waitcnt vmcnt(10)\n\t"
      "v_mfma_f32_16x16x32_bf16 v[168:171], v[128:131], v[68:71], v[168:171]\n\t"
      "v_mfma_f32_16x16x32_bf16 v[172:175], v[132:135], v[68:71], v[172:175]\n\t"
      "s_waitcnt vmcnt(9)\n\t"
      "v_mfma_f32_16x16x32_bf16 v[176:179], v[128:131], v[72:75], v[176:179]\n\t"
      "v_mfma_f32_16x16x32_bf16 v[180:183], v[132:135], v[72:75], v[180:183]\n\t"
      "s_waitcnt lgkmcnt(6)\n\t"
      "v_pk_add_f32 v[208:209], v[208:209], v[212:213]\n\t"
      "v_pk_add_f32 v[210:211], v[210:211], v[214:215]\n\t"
      "s_waitcnt lgkmcnt(4)\n\t"
      "v_pk_add_f32 v[216:217], v[216:217], v[220:221]\n\t"
      "v_pk_add_f32 v[218:219], v[218:219], v[222:223]\n\t"
      "v_pk_add_f32 v[208:209], v[208:209], v[216:217]\n\t"
      "v_pk_add_f32 v[210:211], v[210:211], v[218:219]\n\t"
      "s_waitcnt vmcnt(8)\n\t"
      "v_mfma_f32_16x16x32_bf16 v[160:163], v[136:139], v[76:79], v[160:163]\n\t"
      "v_mfma_f32_16x16x32_bf16 v[164:167], v[140:143], v[76:79], v[164:167]\n\t"
      "s_mov_b32 s80, 0x3dd53b95\n\t"
      "v_max3_f32 v212, v208, v209, v210\n\t"
      "v_max_f32 v212, v212, v211\n\t"
      "v_mov_b32 v213, v212\n\t"
      "s_mov_b32 s82, 0x11111111\n\t"
      "s_mov_b32 s83, 0x22222222\n\t"
      "v_permlane16_swap_b32 v212, v213\n\t"
      "v_max_f32 v212, v212, v213\n\t"
      "s_waitcnt vmcnt(7)\n\t"
      "v_mfma_f32_16x16x32_bf16 v[168:171], v[136:139], v[80:83], v[168:171]\n\t"
      "v_mfma_f32_16x16x32_bf16 v[172:175], v[140:143], v[80:83], v[172:175]\n\t"
      "v_mul_f32 v212, s80, v212\n\t"
      "v_fma_f32 v208, v208, s80, -v212\n\t"
      "v_fma_f32 v209, v209, s80, -v212\n\t"
      "v_fma_f32 v210, v210, s80, -v212\n\t"
      "v_fma_f32 v211, v211, s80, -v212\n\t"
      "v_exp_f32 v208, v208\n\t"
      "v_exp_f32 v209, v209\n\t"
      "v_exp_f32 v210, v210\n\t"
      "s_waitcnt vmcnt(6)\n\t"
      "v_mfma_f32_16x16x32_bf16 v[176:179], v[136:139], v[84:87], v[176:179]\n\t"
      "v_mfma_f32_16x16x32_bf16 v[180:183], v[140:143], v[84:87], v[180:183]\n\t"
      "v_exp_f32 v211, v211\n\t"
      "v_add_f32 v212, v208, v209\n\t"
      "v_add_f32 v213, v210, v211\n\t"
      "v_add_f32 v212, v212, v213\n\t"
      "v_mov_b32 v213, v212\n\t"
      "s_lshl_b64 s[86:87], s[82:83], 2\n\t"
      "s_mov_b32 s88, 0\n\t"
      "v_permlane16_swap_b32 v212, v213\n\t"
      "s_waitcnt vmcnt(5)\n\t"
      "v_mfma_f32_16x16x32_bf16 v[160:163], v[144:147], v[88:91], v[160:163]\n\t"
      "v_mfma_f32_16x16x32_bf16 v[164:167], v[148:151], v[88:91], v[164:167]\n\t"
      "v_add_f32 v212, v212, v213\n\t"
      "v_rcp_f32 v212, v212\n\t"
      "s_mov_b32 s89, -1\n\t"
      "v_mul_f32 v208, v212, v208\n\t"
      "v_mul_f32 v209, v212, v209\n\t"
      "v_mul_f32 v210, v212, v210\n\t"
      "v_mul_f32 v211, v212, v211\n\t"
      "v_cvt_pk_bf16_f32 v214, v208, v209\n\t"
      "s_waitcnt vmcnt(4)\n\t"
      "v_mfma_f32_16x16x32_bf16 v[168:171], v[144:147], v[92:95], v[168:171]\n\t"
      "v_mfma_f32_16x16x32_bf16 v[172:175], v[148:151], v[92:95], v[172:175]\n\t"
      "v_cvt_pk_bf16_f32 v215, v210, v211\n\t"
      "v_mov_b32 v216, v214\n\t"
      "v_mov_b32 v217, v215\n\t"
      "v_cndmask_b32 v236, 0, v214, s[82:83]\n\t"
      "v_cndmask_b32 v237, 0, v215, s[82:83]\n\t"
      "v_cndmask_b32 v238, 0, v214, s[86:87]\n\t"
      "v_cndmask_b32 v239, 0, v215, s[86:87]\n\t"
      "v_permlane32_swap_b32 v214, v216\n\t"
      "s_waitcnt vmcnt(3)\n\t"
      "v_mfma_f32_16x16x32_bf16 v[176:179], v[144:147], v[96:99], v[176:179]\n\t"
      "v_mfma_f32_16x16x32_bf16 v[180:183], v[148:151], v[96:99], v[180:183]\n\t"
      "v_permlane32_swap_b32 v215, v217\n\t"
      "v_cndmask_b32 v214, v216, v214, s[88:89]\n\t"
      "v_cndmask_b32 v215, v217, v215, s[88:89]\n\t"
      "v_cndmask_b32 v240, 0, v214, s[82:83]\n\t"
      "v_cndmask_b32 v241, 0, v215, s[82:83]\n\t"
      "v_cndmask_b32 v242, 0, v214, s[86:87]\n\t"
      "v_cndmask_b32 v243, 0, v215, s[86:87]\n\t"
      "s_waitcnt lgkmcnt(0)\n\t"
      "s_waitcnt vmcnt(2)\n\t"
      "v_mfma_f32_16x16x32_bf16 v[160:163], v[152:155], v[100:103], v[160:163]\n\t"
      "v_mfma_f32_16x16x32_bf16 v[164:167], v[156:159], v[100:103], v[164:167]\n\t"
      "v_lshlrev_b32 v184, 16, v224\n\t"
      "v_and_b32 v185, s94, v224\n\t"
      "v_lshlrev_b32 v186, 16, v225\n\t"
      "v_and_b32 v187, s94, v225\n\t"
      "v_lshlrev_b32 v188, 16, v226\n\t"
      "v_and_b32 v189, s94, v226\n\t"
      "v_lshlrev_b32 v190, 16, v227\n\t"
      "v_and_b32 v191, s94, v227\n\t"
      "s_waitcnt vmcnt(1)\n\t"
      "v_mfma_f32_16x16x32_bf16 v[168:171], v[152:155], v[104:107], v[168:171]\n\t"
      "v_mfma_f32_16x16x32_bf16 v[172:175], v[156:159], v[104:107], v[172:175]\n\t"
      "v_lshlrev_b32 v192, 16, v228\n\t"
      "v_and_b32 v193, s94, v228\n\t"
      "v_lshlrev_b32 v194, 16, v229\n\t"
      "v_and_b32 v195, s94, v229\n\t"
      "v_lshlrev_b32 v196, 16, v230\n\t"
      "v_and_b32 v197, s94, v230\n\t"
      "v_lshlrev_b32 v198, 16, v231\n\t"
      "v_and_b32 v199, s94, v231\n\t"
      "s_waitcnt vmcnt(0)\n\t"
      "v_mfma_f32_16x16x32_bf16 v[176:179], v[152:155], v[108:111], v[176:179]\n\t"
      "v_mfma_f32_16x16x32_bf16 v[180:183], v[156:159], v[108:111], v[180:183]\n\t"
      "v_lshlrev_b32 v200, 16, v232\n\t"
      "v_and_b32 v201, s94, v232\n\t"
      "v_lshlrev_b32 v202, 16, v233\n\t"
      "v_and_b32 v203, s94, v233\n\t"
      "v_lshlrev_b32 v204, 16, v234\n\t"
      "v_and_b32 v205, s94, v234\n\t"
      "v_lshlrev_b32 v206, 16, v235\n\t"
      "v_and_b32 v207, s94, v235\n\t"
      "v_cvt_pk_bf16_f32 v40, v160, v161\n\t"
      "v_cvt_pk_bf16_f32 v41, v162, v163\n\t"
      "v_cvt_pk_bf16_f32 v42, v164, v165\n\t"
      "v_cvt_pk_bf16_f32 v43, v166, v167\n\t"
      "v_cvt_pk_bf16_f32 v44, v168, v169\n\t"
      "v_cvt_pk_bf16_f32 v45, v170, v171\n\t"
      "v_mfma_f32_16x16x32_bf16 v[184:187], v[40:43], v[236:239], v[184:187]\n\t"
      "v_cvt_pk_bf16_f32 v46, v172, v173\n\t"
      "v_cvt_pk_bf16_f32 v47, v174, v175\n\t"
      "v_mfma_f32_16x16x32_bf16 v[196:199], v[40:43], v[240:243], v[196:199]\n\t"
      "v_cvt_pk_bf16_f32 v48, v176, v177\n\t"
      "v_cvt_pk_bf16_f32 v49, v178, v179\n\t"
      "v_mfma_f32_16x16x32_bf16 v[188:191], v[44:47], v[236:239], v[188:191]\n\t"
      "v_cvt_pk_bf16_f32 v50, v180, v181\n\t"
      "v_cvt_pk_bf16_f32 v51, v182, v183\n\t"
      "v_mfma_f32_16x16x32_bf16 v[200:203], v[44:47], v[240:243], v[200:203]\n\t"
      "s_nop 0\n\t"
      "v_mfma_f32_16x16x32_bf16 v[192:195], v[48:51], v[236:239], v[192:195]\n\t"
      "v_mfma_f32_16x16x32_bf16 v[204:207], v[48:51], v[240:243], v[204:207]\n\t"
      "s_nop 2\n\t"
      "v_pk_add_f32 v[56:57], v[184:185], v[186:187]\n\t"
      "v_pk_mul_f32 v[58:59], v[184:185], v[184:185]\n\t"
      "v_pk_add_f32 v[60:61], v[196:197], v[198:199]\n\t"
      "v_pk_mul_f32 v[62:63], v[196:197], v[196:197]\n\t"
      "v_pk_fma_f32 v[58:59], v[186:187], v[186:187], v[58:59]\n\t"
      "v_pk_fma_f32 v[62:63], v[198:199], v[198:199], v[62:63]\n\t"
      "v_pk_add_f32 v[56:57], v[56:57], v[188:189]\n\t"
      "v_pk_fma_f32 v[58:59], v[188:189], v[188:189], v[58:59]\n\t"
      "v_pk_add_f32 v[60:61], v[60:61], v[200:201]\n\t"
      "v_pk_fma_f32 v[62:63], v[200:201], v[200:201], v[62:63]\n\t"
      "v_pk_add_f32 v[56:57], v[56:57], v[190:191]\n\t"
      "v_pk_fma_f32 v[58:59], v[190:191], v[190:191], v[58:59]\n\t"
      "v_pk_add_f32 v[60:61], v[60:61], v[202:203]\n\t"
      "v_pk_fma_f32 v[62:63], v[202:203], v[202:203], v[62:63]\n\t"
      "v_pk_add_f32 v[56:57], v[56:57], v[192:193]\n\t"
      "v_pk_fma_f32 v[58:59], v[192:193], v[192:193], v[58:59]\n\t"
      "v_pk_add_f32 v[60:61], v[60:61], v[204:205]\n\t"
      "v_pk_fma_f32 v[62:63], v[204:205], v[204:205], v[62:63]\n\t"
      "v_pk_add_f32 v[56:57], v[56:57], v[194:195]\n\t"
      "v_pk_fma_f32 v[58:59], v[194:195], v[194:195], v[58:59]\n\t"
      "v_pk_add_f32 v[60:61], v[60:61], v[206:207]\n\t"
      "v_pk_fma_f32 v[62:63], v[206:207], v[206:207], v[62:63]\n\t"
      "v_add_f32 v56, v56, v57\n\t"
      "v_add_f32 v60, v60, v61\n\t"
      "v_add_f32 v58, v58, v59\n\t"
      "v_add_f32 v62, v62, v63\n\t"
      "v_permlane32_swap_b32 v56, v60\n\t"
      "s_nop 0\n\t"
      "v_permlane32_swap_b32 v58, v62\n\t"
      "v_add_f32 v56, v56, v60\n\t"
      "v_add_f32 v58, v58, v62\n\t"
      "s_nop 1\n\t"
      "v_permlane16_swap_b32 v56, v58\n\t"
      "v_add_f32 v56, v56, v58\n\t"
      "ds_write_b32 v16, v56\n\t"
      "s_mov_b32 s80, 0x3baaaaab\n\t"
      "s_mov_b32 s81, s80\n\t"
      "s_cmp_eq_u32 %[w], 5\n\t"
      "s_waitcnt lgkmcnt(0)\n\t"
      "s_barrier\n\t"
      "ds_read_b128 v[56:59], v17 offset:0\n\t"
      "ds_read_b128 v[60:63], v17 offset:16\n\t"
      "ds_read_b128 v[64:67], v17 offset:512\n\t"
      "ds_read_b128 v[68:71], v17 offset:528\n\t"
      "s_waitcnt lgkmcnt(2)\n\t"
      "v_pk_add_f32 v[56:57], v[56:57], v[58:59]\n\t"
      "v_pk_add_f32 v[60:61], v[60:61], v[62:63]\n\t"
      "s_waitcnt lgkmcnt(0)\n\t"
      "v_pk_add_f32 v[64:65], v[64:65], v[66:67]\n\t"
      "v_pk_add_f32 v[68:69], v[68:69], v[70:71]\n\t"
      "v_pk_add_f32 v[56:57], v[56:57], v[60:61]\n\t"
      "v_pk_add_f32 v[64:65], v[64:65], v[68:69]\n\t"
      "v_pk_mul_f32 v[56:57], v[56:57], s[80:81]\n\t"
      "v_pk_mul_f32 v[64:65], v[64:65], s[80:81]\n\t"
      "v_fma_f32 v57, -v56, v56, v57\n\t"
      "v_fma_f32 v65, -v64, v64, v65\n\t"
      "v_max_f32 v57, 0, v57\n\t"
      "v_max_f32 v65, 0, v65\n\t"
      "v_add_f32 v57, 0x3727c5ac, v57\n\t"
      "v_add_f32 v65, 0x3727c5ac, v65\n\t"
      "v_rsq_f32 v58, v57\n\t"
      "v_rsq_f32 v66, v65\n\t"
      "v_mul_f32 v60, v56, v58\n\t"
      "v_mul_f32 v68, v64, v66\n\t"
      "v_pk_fma_f32 v[184:185], v[184:185], v[58:59], v[60:61] op_sel_hi:[1,0,0] neg_lo:[0,0,1] neg_hi:[0,0,1]\n\t"
      "v_pk_fma_f32 v[186:187], v[186:187], v[58:59], v[60:61] op_sel_hi:[1,0,0] neg_lo:[0,0,1] neg_hi:[0,0,1]\n\t"
      "v_pk_fma_f32 v[188:189], v[188:189], v[58:59], v[60:61] op_sel_hi:[1,0,0] neg_lo:[0,0,1] neg_hi:[0,0,1]\n\t"
      "v_pk_fma_f32 v[190:191], v[190:191], v[58:59], v[60:61] op_sel_hi:[1,0,0] neg_lo:[0,0,1] neg_hi:[0,0,1]\n\t"
      "v_cvt_pk_bf16_f32 v40, v184, v185\n\t"
      "v_cvt_pk_bf16_f32 v41, v186, v187\n\t"
      "v_cvt_pk_bf16_f32 v42, v188, v189\n\t"
      "v_cvt_pk_bf16_f32 v43, v190, v191\n\t"
      "global_store_dwordx4 v18, v[40:43], s[74:75] sc1\n\t"
      "v_pk_fma_f32 v[196:197], v[196:197], v[66:67], v[68:69] op_sel_hi:[1,0,0] neg_lo:[0,0,1] neg_hi:[0,0,1]\n\t"
      "v_pk_fma_f32 v[198:199], v[198:199], v[66:67], v[68:69] op_sel_hi:[1,0,0] neg_lo:[0,0,1] neg_hi:[0,0,1]\n\t"
      "v_pk_fma_f32 v[200:201], v[200:201], v[66:67], v[68:69] op_sel_hi:[1,0,0] neg_lo:[0,0,1] neg_hi:[0,0,1]\n\t"
      "v_pk_fma_f32 v[202:203], v[202:203], v[66:67], v[68:69] op_sel_hi:[1,0,0] neg_lo:[0,0,1] neg_hi:[0,0,1]\n\t"
      "v_cvt_pk_bf16_f32 v44, v196, v197\n\t"
      "v_cvt_pk_bf16_f32 v45, v198, v199\n\t"
      "v_cvt_pk_bf16_f32 v46, v200, v201\n\t"
      "v_cvt_pk_bf16_f32 v47, v202, v203\n\t"
      "global_store_dwordx4 v18, v[44:47], s[62:63] sc1\n\t"
      "v_pk_fma_f32 v[192:193], v[192:193], v[58:59], v[60:61] op_sel_hi:[1,0,0] neg_lo:[0,0,1] neg_hi:[0,0,1]\n\t"
      "v_pk_fma_f32 v[194:195], v[194:195], v[58:59], v[60:61] op_sel_hi:[1,0,0] neg_lo:[0,0,1] neg_hi:[0,0,1]\n\t"
      "v_pk_fma_f32 v[204:205], v[204:205], v[66:67], v[68:69] op_sel_hi:[1,0,0] neg_lo:[0,0,1] neg_hi:[0,0,1]\n\t"
      "v_pk_fma_f32 v[206:207], v[206:207], v[66:67], v[68:69] op_sel_hi:[1,0,0] neg_lo:[0,0,1] neg_hi:[0,0,1]\n\t"
      "v_cvt_pk_bf16_f32 v48, v192, v193\n\t"
      "v_cvt_pk_bf16_f32 v50, v204, v205\n\t"
      "v_cvt_pk_bf16_f32 v49, v194, v195\n\t"
      "v_cvt_pk_bf16_f32 v51, v206, v207\n\t"
      "v_permlane16_swap_b32 v48, v50\n\t"
      "s_nop 0\n\t"
      "v_permlane16_swap_b32 v49, v51\n\t"
      "global_store_dwordx4 v19, v[48:51], s[74:75] offset:2048 sc1\n\t"
      "s_cbranch_scc0 59f\n\t"
      "v_mul_f32 v57, v57, v58\n\t"
      "v_mul_f32 v65, v65, v66\n\t"
      "s_mov_b32 s95, s94\n\t"
      "v_cndmask_b32 v56, v56, v64, s[94:95]\n\t"
      "v_cndmask_b32 v57, v57, v65, s[94:95]\n\t"
      "s_mov_b32 exec_hi, 0\n\t"
      "global_store_dwordx2 v34, v[56:57], s[74:75] sc1\n\t"
      "s_mov_b64 exec, -1\n\t"
      "s_branch 59f\n\t"
      "70:\n\t"
      "v_lshrrev_b32 v4, 5, v2\n\t"
      "v_and_b32 v5, 31, v2\n\t"
      "v_bfe_u32 v6, v2, 2, 2\n\t"
      "v_bfe_u32 v10, v2, 4, 1\n\t"
      "v_lshl_add_u32 v7, v4, 3, v6\n\t"
      "s_movk_i32 s80, 768\n\t"
      "v_mul_u32_u24 v7, s80, v7\n\t"
      "v_and_b32 v8, 3, v2\n\t"
      "v_lshl_add_u32 v7, v8, 3, v7\n\t"
      "v_lshl_add_u32 v6, v6, 1, v10\n\t"
      "v_mul_u32_u24 v10, 12, v10\n\t"
      "s_mul_i32 s80, %[w], 3\n\t"
      "v_add3_u32 v8, v10, s80, 0\n\t"
      "v_xor_b32 v8, v8, v6\n\t"
      "v_lshl_add_u32 v12, v8, 5, v7\n\t"
      "v_add3_u32 v8, v10, s80, 1\n\t"
      "v_xor_b32 v8, v8, v6\n\t"
      "v_lshl_add_u32 v13, v8, 5, v7\n\t"
      "v_add3_u32 v8, v10, s80, 2\n\t"
      "v_xor_b32 v8, v8, v6\n\t"
      "v_lshl_add_u32 v14, v8, 5, v7\n\t"
      "s_movk_i32 s80, 528\n\t"
      "v_lshlrev_b32 v8, 4, v5\n\t"
      "v_mad_u32_u24 v8, v4, s80, v8\n\t"
      "v_add_u32 v15, 147456, v8\n\t"
      "v_and_b32 v8, 7, v2\n\t"
      "v_mul_u32_u24 v9, s80, v8\n\t"
      "v_lshrrev_b32 v10, 3, v2\n\t"
      "v_lshl_add_u32 v9, v10, 4, v9\n\t"
      "s_lshl_b32 s81, %[w], 7\n\t"
      "s_add_u32 s81, s81, 36864\n\t"
      "v_add_u32 v16, s81, v9\n\t"
      "v_add_u32 v17, 110592, v16\n\t"
      "s_lshl_b32 s81, %[w], 10\n\t"
      "v_add_u32 v19, s81, v3\n\t"
      "v_lshrrev_b32 v8, 1, v2\n\t"
      "v_and_b32 v7, 1, v2\n\t"
      "v_mad_u32_u24 v7, v8, 6, v7\n\t"
      "s_mul_i32 s80, %[w], 3\n\t"
      "s_lshr_b32 s80, s80, 1\n\t"
      "s_addk_i32 s80, 144\n\t"
      "v_add_u32 v7, s80, v7\n\t"
      "v_cmp_gt_u32 vcc, 24, v2\n\t"
      "v_min_u32 v8, 41, v2\n\t"
      "s_mul_i32 s80, %[w], 18\n\t"
      "s_addk_i32 s80, 48\n\t"
      "v_add_u32 v8, s80, v8\n\t"
      "v_cndmask_b32 v7, v8, v7, vcc\n\t"
      "s_mov_b32 s80, 3465216\n\t"
      "v_lshl_add_u32 v33, v7, 7, s80\n\t"
      "s_mul_i32 s80, %[w], 0x4800\n\t"
      "s_add_u32 s80, s80, 3222528\n\t"
      "s_add_u32 s68, s74, s80\n\t"
      "s_addc_u32 s69, s75, 0\n\t"
      "s_add_u32 s70, s68, 0x1800\n\t"
      "s_addc_u32 s71, s69, 0\n\t"
      "s_add_u32 s72, s68, 0x3000\n\t"
      "s_addc_u32 s73, s69, 0\n\t"
      "v_mov_b32 v40, 0\n\t"
      "v_mov_b32 v41, 0\n\t"
      "v_mov_b32 v42, 0\n\t"
      "v_mov_b32 v43, 0\n\t"
      "v_mov_b32 v44, 0\n\t"
      "v_mov_b32 v45, 0\n\t"
      "v_mov_b32 v46, 0\n\t"
      "v_mov_b32 v47, 0\n\t"
      "v_mov_b32 v48, 0\n\t"
      "v_mov_b32 v49, 0\n\t"
      "v_mov_b32 v50, 0\n\t"
      "v_mov_b32 v51, 0\n\t"
      "v_mov_b32 v52, 0\n\t"
      "v_mov_b32 v53, 0\n\t"
      "v_mov_b32 v54, 0\n\t"
      "v_mov_b32 v55, 0\n\t"
      "v_mov_b32 v56, 0\n\t"
      "v_mov_b32 v57, 0\n\t"
      "v_mov_b32 v58, 0\n\t"
      "v_mov_b32 v59, 0\n\t"
      "v_mov_b32 v60, 0\n\t"
      "v_mov_b32 v61, 0\n\t"
      "v_mov_b32 v62, 0\n\t"
      "v_mov_b32 v63, 0\n\t"
      "v_mov_b32 v64, 0\n\t"
      "v_mov_b32 v65, 0\n\t"
      "v_mov_b32 v66, 0\n\t"
      "v_mov_b32 v67, 0\n\t"
      "v_mov_b32 v68, 0\n\t"
      "v_mov_b32 v69, 0\n\t"
      "v_mov_b32 v70, 0\n\t"
      "v_mov_b32 v71, 0\n\t"
      "v_mov_b32 v72, 0\n\t"
      "v_mov_b32 v73, 0\n\t"
      "v_mov_b32 v74, 0\n\t"
      "v_mov_b32 v75, 0\n\t"
      "v_mov_b32 v76, 0\n\t"
      "v_mov_b32 v77, 0\n\t"
      "v_mov_b32 v78, 0\n\t"
      "v_mov_b32 v79, 0\n\t"
      "v_mov_b32 v80, 0\n\t"
      "v_mov_b32 v81, 0\n\t"
      "v_mov_b32 v82, 0\n\t"
      "v_mov_b32 v83, 0\n\t"
      "v_mov_b32 v84, 0\n\t"
      "v_mov_b32 v85, 0\n\t"
      "v_mov_b32 v86, 0\n\t"
      "v_mov_b32 v87, 0\n\t"
      "s_mov_b32 s92, 0xff00ff00\n\t"
      "s_mov_b32 s93, s92\n\t"
      "s_mov_b32 s94, 0xffff0000\n\t"
      "s_mov_b32 s95, s94\n\t"
      "s_lshr_b32 s85, %[b], 3\n\t"
      "s_waitcnt vmcnt(0)\n\t"
      "v_lshlrev_b32 v108, 16, v20\n\t"
      "v_and_b32 v109, s94, v20\n\t"
      "v_lshlrev_b32 v110, 16, v21\n\t"
      "v_and_b32 v111, s94, v21\n\t"
      "v_lshlrev_b32 v112, 16, v22\n\t"
      "v_and_b32 v113, s94, v22\n\t"
      "v_lshlrev_b32 v114, 16, v23\n\t"
      "v_and_b32 v115, s94, v23\n\t"
      "v_lshlrev_b32 v116, 16, v24\n\t"
      "v_and_b32 v117, s94, v24\n\t"
      "v_lshlrev_b32 v118, 16, v25\n\t"
      "v_and_b32 v119, s94, v25\n\t"
      "v_lshlrev_b32 v120, 16, v26\n\t"
      "v_and_b32 v121, s94, v26\n\t"
      "v_lshlrev_b32 v122, 16, v27\n\t"
      "v_and_b32 v123, s94, v27\n\t"
      "v_lshlrev_b32 v124, 16, v28\n\t"
      "v_and_b32 v125, s94, v28\n\t"
      "v_lshlrev_b32 v126, 16, v29\n\t"
      "v_and_b32 v127, s94, v29\n\t"
      "v_lshlrev_b32 v128, 16, v30\n\t"
      "v_and_b32 v129, s94, v30\n\t"
      "v_lshlrev_b32 v130, 16, v31\n\t"
      "v_and_b32 v131, s94, v31\n\t"
      "v_pk_add_f32 v[4:5], v[108:109], v[110:111]\n\t"
      "v_pk_add_f32 v[6:7], v[112:113], v[114:115]\n\t"
      "v_pk_add_f32 v[4:5], v[4:5], v[116:117]\n\t"
      "v_pk_add_f32 v[6:7], v[6:7], v[118:119]\n\t"
      "v_pk_add_f32 v[4:5], v[4:5], v[120:121]\n\t"
      "v_pk_add_f32 v[6:7], v[6:7], v[122:123]\n\t"
      "v_pk_add_f32 v[4:5], v[4:5], v[124:125]\n\t"
      "v_pk_add_f32 v[6:7], v[6:7], v[126:127]\n\t"
      "v_pk_add_f32 v[4:5], v[4:5], v[128:129]\n\t"
      "v_pk_add_f32 v[6:7], v[6:7], v[130:131]\n\t"
      "s_nop 0\n\t"
      "v_pk_add_f32 v[4:5], v[4:5], v[6:7]\n\t"
      "s_nop 0\n\t"
      "v_add_f32 v4, v4, v5\n\t"
      "s_nop 1\n\t"
      "v_add_f32_dpp v4, v4, v4 quad_perm:[1,0,3,2] row_mask:0xf bank_mask:0xf\n\t"
      "s_nop 1\n\t"
      "v_add_f32_dpp v4, v4, v4 quad_perm:[2,3,0,1] row_mask:0xf bank_mask:0xf\n\t"
      "s_nop 1\n\t"
      "v_add_f32_dpp v4, v4, v4 row_half_mirror row_mask:0xf bank_mask:0xf\n\t"
      "v_mul_f32 v8, 0xbbaaaaab, v4\n\t"
      "v_pk_add_f32 v[108:109], v[108:109], v[8:9] op_sel_hi:[1,0]\n\t"
      "v_pk_add_f32 v[110:111], v[110:111], v[8:9] op_sel_hi:[1,0]\n\t"
      "v_pk_add_f32 v[112:113], v[112:113], v[8:9] op_sel_hi:[1,0]\n\t"
      "v_pk_add_f32 v[114:115], v[114:115], v[8:9] op_sel_hi:[1,0]\n\t"
      "v_pk_add_f32 v[116:117], v[116:117], v[8:9] op_sel_hi:[1,0]\n\t"
      "v_pk_add_f32 v[118:119], v[118:119], v[8:9] op_sel_hi:[1,0]\n\t"
      "v_pk_add_f32 v[120:121], v[120:121], v[8:9] op_sel_hi:[1,0]\n\t"
      "v_pk_add_f32 v[122:123], v[122:123], v[8:9] op_sel_hi:[1,0]\n\t"
      "v_pk_add_f32 v[124:125], v[124:125], v[8:9] op_sel_hi:[1,0]\n\t"
      "v_pk_add_f32 v[126:127], v[126:127], v[8:9] op_sel_hi:[1,0]\n\t"
      "v_pk_add_f32 v[128:129], v[128:129], v[8:9] op_sel_hi:[1,0]\n\t"
      "v_pk_add_f32 v[130:131], v[130:131], v[8:9] op_sel_hi:[1,0]\n\t"
      "v_pk_mul_f32 v[4:5], v[108:109], v[108:109]\n\t"
      "v_pk_mul_f32 v[6:7], v[110:111], v[110:111]\n\t"
      "v_pk_fma_f32 v[4:5], v[112:113], v[112:113], v[4:5]\n\t"
      "v_pk_fma_f32 v[6:7], v[114:115], v[114:115], v[6:7]\n\t"
      "v_pk_fma_f32 v[4:5], v[116:117], v[116:117], v[4:5]\n\t"
      "v_pk_fma_f32 v[6:7], v[118:119], v[118:119], v[6:7]\n\t"
      "v_pk_fma_f32 v[4:5], v[120:121], v[120:121], v[4:5]\n\t"
      "v_pk_fma_f32 v[6:7], v[122:123], v[122:123], v[6:7]\n\t"
      "v_pk_fma_f32 v[4:5], v[124:125], v[124:125], v[4:5]\n\t"
      "v_pk_fma_f32 v[6:7], v[126:127], v[126:127], v[6:7]\n\t"
      "v_pk_fma_f32 v[4:5], v[128:129], v[128:129], v[4:5]\n\t"
      "v_pk_fma_f32 v[6:7], v[130:131], v[130:131], v[6:7]\n\t"
      "s_nop 0\n\t"
      "v_pk_add_f32 v[4:5], v[4:5], v[6:7]\n\t"
      "s_nop 0\n\t"
      "v_add_f32 v4, v4, v5\n\t"
      "s_nop 1\n\t"
      "v_add_f32_dpp v4, v4, v4 quad_perm:[1,0,3,2] row_mask:0xf bank_mask:0xf\n\t"
      "s_nop 1\n\t"
      "v_add_f32_dpp v4, v4, v4 quad_perm:[2,3,0,1] row_mask:0xf bank_mask:0xf\n\t"
      "s_nop 1\n\t"
      "v_add_f32_dpp v4, v4, v4 row_half_mirror row_mask:0xf bank_mask:0xf\n\t"
      "v_mov_b32 v8, 0x3727c5ac\n\t"
      "v_fmac_f32 v8, 0x3baaaaab, v4\n\t"
      "v_rsq_f32 v8, v8\n\t"
      "s_nop 0\n\t"
      "v_pk_mul_f32 v[108:109], v[108:109], v[8:9] op_sel_hi:[1,0]\n\t"
      "v_pk_mul_f32 v[110:111], v[110:111], v[8:9] op_sel_hi:[1,0]\n\t"
      "v_pk_mul_f32 v[112:113], v[112:113], v[8:9] op_sel_hi:[1,0]\n\t"
      "v_pk_mul_f32 v[114:115], v[114:115], v[8:9] op_sel_hi:[1,0]\n\t"
      "v_cvt_pk_bf16_f32 v132, v108, v109\n\t"
      "v_cvt_pk_bf16_f32 v133, v110, v111\n\t"
      "v_cvt_pk_bf16_f32 v134, v112, v113\n\t"
      "v_cvt_pk_bf16_f32 v135, v114, v115\n\t"
      "ds_write_b128 v17, v[132:135] offset:0\n\t"
      "v_pk_mul_f32 v[116:117], v[116:117], v[8:9] op_sel_hi:[1,0]\n\t"
      "v_pk_mul_f32 v[118:119], v[118:119], v[8:9] op_sel_hi:[1,0]\n\t"
      "v_pk_mul_f32 v[120:121], v[120:121], v[8:9] op_sel_hi:[1,0]\n\t"
      "v_pk_mul_f32 v[122:123], v[122:123], v[8:9] op_sel_hi:[1,0]\n\t"
      "v_cvt_pk_bf16_f32 v136, v116, v117\n\t"
      "v_cvt_pk_bf16_f32 v137, v118, v119\n\t"
      "v_cvt_pk_bf16_f32 v138, v120, v121\n\t"
      "v_cvt_pk_bf16_f32 v139, v122, v123\n\t"
      "ds_write_b128 v17, v[136:139] offset:4224\n\t"
      "v_pk_mul_f32 v[124:125], v[124:125], v[8:9] op_sel_hi:[1,0]\n\t"
      "v_pk_mul_f32 v[126:127], v[126:127], v[8:9] op_sel_hi:[1,0]\n\t"
      "v_pk_mul_f32 v[128:129], v[128:129], v[8:9] op_sel_hi:[1,0]\n\t"
      "v_pk_mul_f32 v[130:131], v[130:131], v[8:9] op_sel_hi:[1,0]\n\t"
      "v_cvt_pk_bf16_f32 v140, v124, v125\n\t"
      "v_cvt_pk_bf16_f32 v141, v126, v127\n\t"
      "v_cvt_pk_bf16_f32 v142, v128, v129\n\t"
      "v_cvt_pk_bf16_f32 v143, v130, v131\n\t"
      "ds_write_b128 v17, v[140:143] offset:8448\n\t"
      "v_readfirstlane_b32 s96, v32\n\t"
      "s_nop 0\n\t"
      "s_mul_i32 s98, s96, 0x9E3779B1\n\t"
      "s_add_u32 s98, s98, 0x7F4A7C15\n\t"
      "s_xor_b32 s99, s98, 0x5bd1e995\n\t"
      "s_and_b32 s80, s85, 3\n\t"
      "s_mul_i32 s80, s80, 3168\n\t"
      "s_add_u32 s85, s85, 1\n\t"
      "v_add_u32 v18, s80, v15\n\t"
      "s_mov_b32 s97, 3\n\t"
      "s_waitcnt lgkmcnt(0)\n\t"
      "60:\n\t"
      "s_barrier\n\t"
      "ds_read_b128 v[88:91], v18 offset:0\n\t"
      "ds_read_b64_tr_b16 v[100:101], v12 offset:0\n\t"
      "ds_read_b64_tr_b16 v[102:103], v12 offset:3072\n\t"
      "ds_read_b64_tr_b16 v[104:105], v13 offset:0\n\t"
      "ds_read_b64_tr_b16 v[106:107], v13 offset:3072\n\t"
      "ds_read_b64_tr_b16 v[108:109], v14 offset:0\n\t"
      "ds_read_b64_tr_b16 v[110:111], v14 offset:3072\n\t"
      "ds_read_b128 v[92:95], v18 offset:1056\n\t"
      "ds_read_b64_tr_b16 v[112:113], v12 offset:12288\n\t"
      "ds_read_b64_tr_b16 v[114:115], v12 offset:15360\n\t"
      "ds_read_b64_tr_b16 v[116:117], v13 offset:12288\n\t"
      "ds_read_b64_tr_b16 v[118:119], v13 offset:15360\n\t"
      "ds_read_b64_tr_b16 v[120:121], v14 offset:12288\n\t"
      "ds_read_b64_tr_b16 v[122:123], v14 offset:15360\n\t"
      "s_waitcnt lgkmcnt(11)\n\t"
      "v_mfma_f32_32x32x16_bf16 v[40:55], v[100:103], v[88:91], v[40:55]\n\t"
      "ds_read_b128 v[96:99], v18 offset:2112\n\t"
      "ds_read_b64_tr_b16 v[124:125], v12 offset:24576\n\t"
      "ds_read_b64_tr_b16 v[126:127], v12 offset:27648\n\t"
      "s_waitcnt lgkmcnt(12)\n\t"
      "v_mfma_f32_32x32x16_bf16 v[56:71], v[104:107], v[88:91], v[56:71]\n\t"
      "ds_read_b64_tr_b16 v[128:129], v13 offset:24576\n\t"
      "ds_read_b64_tr_b16 v[130:131], v13 offset:27648\n\t"
      "s_waitcnt lgkmcnt(12)\n\t"
      "v_mfma_f32_32x32x16_bf16 v[72:87], v[108:111], v[88:91], v[72:87]\n\t"
      "ds_read_b64_tr_b16 v[132:133], v14 offset:24576\n\t"
      "ds_read_b64_tr_b16 v[134:135], v14 offset:27648\n\t"
      "s_waitcnt lgkmcnt(11)\n\t"
      "v_mfma_f32_32x32x16_bf16 v[40:55], v[112:115], v[92:95], v[40:55]\n\t"
      "s_and_b32 s80, s85, 3\n\t"
      "s_mul_i32 s80, s80, 3168\n\t"
      "s_waitcnt lgkmcnt(9)\n\t"
      "v_mfma_f32_32x32x16_bf16 v[56:71], v[116:119], v[92:95], v[56:71]\n\t"
      "s_add_u32 s85, s85, 1\n\t"
      "v_add_u32 v18, s80, v15\n\t"
      "s_waitcnt lgkmcnt(7)\n\t"
      "v_mfma_f32_32x32x16_bf16 v[72:87], v[120:123], v[92:95], v[72:87]\n\t"
      "v_add_u32 v12, 36864, v12\n\t"
      "v_add_u32 v13, 36864, v13\n\t"
      "s_waitcnt lgkmcnt(4)\n\t"
      "v_mfma_f32_32x32x16_bf16 v[40:55], v[124:127], v[96:99], v[40:55]\n\t"
      "v_add_u32 v14, 36864, v14\n\t"
      "s_sub_u32 s97, s97, 1\n\t"
      "s_waitcnt lgkmcnt(2)\n\t"
      "v_mfma_f32_32x32x16_bf16 v[56:71], v[128:131], v[96:99], v[56:71]\n\t"
      "s_waitcnt lgkmcnt(0)\n\t"
      "v_mfma_f32_32x32x16_bf16 v[72:87], v[132:135], v[96:99], v[72:87]\n\t"
      "s_cbranch_scc0 60b\n\t"
      "global_load_dwordx2 v[38:39], v33, s[74:75] sc1\n\t"
      "ds_write_b128 v16, v[20:23] offset:0\n\t"
      "ds_write_b128 v16, v[24:27] offset:4224\n\t"
      "ds_write_b128 v16, v[28:31] offset:8448\n\t"
      "s_nop 3\n\t"
      "v_cvt_pk_bf16_f32 v88, v40, v41\n\t"
      "v_cvt_pk_bf16_f32 v89, v42, v43\n\t"
      "v_cvt_pk_bf16_f32 v90, v44, v45\n\t"
      "v_cvt_pk_bf16_f32 v91, v46, v47\n\t"
      "v_cvt_pk_bf16_f32 v92, v48, v49\n\t"
      "v_cvt_pk_bf16_f32 v93, v50, v51\n\t"
      "v_cvt_pk_bf16_f32 v94, v52, v53\n\t"
      "v_cvt_pk_bf16_f32 v95, v54, v55\n\t"
      "v_cvt_pk_bf16_f32 v96, v56, v57\n\t"
      "v_cvt_pk_bf16_f32 v97, v58, v59\n\t"
      "v_mfma_f32_32x32x16_bf16 v[136:151], v[92:95], v[88:91], 0\n\t"
      "v_cvt_pk_bf16_f32 v98, v60, v61\n\t"
      "v_cvt_pk_bf16_f32 v99, v62, v63\n\t"
      "v_cvt_pk_bf16_f32 v100, v64, v65\n\t"
      "v_cvt_pk_bf16_f32 v101, v66, v67\n\t"
      "v_cvt_pk_bf16_f32 v102, v68, v69\n\t"
      "v_cvt_pk_bf16_f32 v103, v70, v71\n\t"
      "v_cvt_pk_bf16_f32 v104, v72, v73\n\t"
      "v_cvt_pk_bf16_f32 v105, v74, v75\n\t"
      "v_mfma_f32_32x32x16_bf16 v[136:151], v[100:103], v[96:99], v[136:151]\n\t"
      "v_cvt_pk_bf16_f32 v106, v76, v77\n\t"
      "v_cvt_pk_bf16_f32 v107, v78, v79\n\t"
      "v_cvt_pk_bf16_f32 v108, v80, v81\n\t"
      "v_cvt_pk_bf16_f32 v109, v82, v83\n\t"
      "v_cvt_pk_bf16_f32 v110, v84, v85\n\t"
      "v_cvt_pk_bf16_f32 v111, v86, v87\n\t"
      "s_nop 1\n\t"
      "v_mfma_f32_32x32x16_bf16 v[136:151], v[108:111], v[104:107], v[136:151]\n\t"
      "s_nop 11\n\t"
      "v_cndmask_b32 v112, v136, v140, s[92:93]\n\t"
      "v_cndmask_b32 v116, v144, v148, s[92:93]\n\t"
      "v_cndmask_b32 v113, v137, v141, s[92:93]\n\t"
      "v_cndmask_b32 v117, v145, v149, s[92:93]\n\t"
      "v_cndmask_b32 v114, v138, v142, s[92:93]\n\t"
      "v_cndmask_b32 v118, v146, v150, s[92:93]\n\t"
      "v_cndmask_b32 v115, v139, v143, s[92:93]\n\t"
      "v_cndmask_b32 v119, v147, v151, s[92:93]\n\t"
      "v_cndmask_b32 v112, v112, v116, s[94:95]\n\t"
      "v_cndmask_b32 v113, v113, v117, s[94:95]\n\t"
      "v_cndmask_b32 v114, v114, v118, s[94:95]\n\t"
      "v_cndmask_b32 v115, v115, v119, s[94:95]\n\t"
      "ds_write_b128 v19, v[112:115]\n\t"
      "s_waitcnt lgkmcnt(0)\n\t"
      "s_barrier\n\t"
      "s_mov_b32 s97, 0x10000\n\t"
      "61:\n\t"
      "s_waitcnt vmcnt(0)\n\t"
      "v_cmp_ne_u64 vcc, s[98:99], v[38:39]\n\t"
      "s_cbranch_vccz 62f\n\t"
      "s_sleep 2\n\t"
      "global_load_dwordx2 v[38:39], v33, s[74:75] sc1\n\t"
      "s_sub_u32 s97, s97, 1\n\t"
      "s_cbranch_scc0 61b\n\t"
      "62:\n\t"
      "global_load_dwordx4 v[160:163], v3, s[68:69] offset:-3072 sc1\n\t"
      "global_load_dwordx4 v[164:167], v3, s[68:69] offset:-2048 sc1\n\t"
      "global_load_dwordx4 v[168:171], v3, s[68:69] offset:-1024 sc1\n\t"
      "global_load_dwordx4 v[172:175], v3, s[68:69] offset:0 sc1\n\t"
      "global_load_dwordx4 v[176:179], v3, s[68:69] offset:1024 sc1\n\t"
      "global_load_dwordx4 v[180:183], v3, s[68:69] offset:2048 sc1\n\t"
      "global_load_dwordx4 v[184:187], v3, s[70:71] offset:-3072 sc1\n\t"
      "global_load_dwordx4 v[188:191], v3, s[70:71] offset:-2048 sc1\n\t"
      "global_load_dwordx4 v[192:195], v3, s[70:71] offset:-1024 sc1\n\t"
      "global_load_dwordx4 v[196:199], v3, s[70:71] offset:0 sc1\n\t"
      "global_load_dwordx4 v[200:203], v3, s[70:71] offset:1024 sc1\n\t"
      "global_load_dwordx4 v[204:207], v3, s[70:71] offset:2048 sc1\n\t"
      "global_load_dwordx4 v[208:211], v3, s[72:73] offset:-3072 sc1\n\t"
      "global_load_dwordx4 v[212:215], v3, s[72:73] offset:-2048 sc1\n\t"
      "global_load_dwordx4 v[216:219], v3, s[72:73] offset:-1024 sc1\n\t"
      "global_load_dwordx4 v[220:223], v3, s[72:73] offset:0 sc1\n\t"
      "global_load_dwordx4 v[224:227], v3, s[72:73] offset:1024 sc1\n\t"
      "global_load_dwordx4 v[228:231], v3, s[72:73] offset:2048 sc1\n\t"
      "s_barrier\n\t"
      "s_waitcnt vmcnt(0)\n\t"
      "59:\n\t"
      "s_nop 1\n\t"
      : "=&{s[74:75]}"(wsp), "=&{s[76:77]}"(outp), "=&{v32}"(xv), "=&{v[160:191]}"(m0), "=&{v[192:223]}"(m1), "=&{v[224:231]}"(m2)
      : [ka] "s"(__builtin_amdgcn_kernarg_segment_ptr()), [tid] "v"(threadIdx.x), [w] "s"(w), [b] "s"(b)
      : "memory", "scc", "vcc", "s58", "s59", "s60", "s61", "s62", "s63", "s64", "s65", "s66", "s67", "s68", "s69", "s70", "s71", "s72", "s73", "s78", "s79", "s80", "s81", "s82", "s83", "s84", "s85", "s86", "s87", "s88", "s89", "s90", "s91", "s92", "s93", "s94", "s95", "s96", "s97", "s98", "s99", "v2", "v3", "v4", "v5", "v6", "v7", "v8", "v9", "v10", "v11", "v12", "v13", "v14", "v15", "v16", "v17", "v18", "v19", "v20", "v21", "v22", "v23", "v24", "v25", "v26", "v27", "v28", "v29", "v30", "v31", "v33", "v34", "v35", "v36", "v37", "v38", "v39", "v40", "v41", "v42", "v43", "v44", "v45", "v46", "v47", "v48", "v49", "v50", "v51", "v52", "v53", "v54", "v55", "v56", "v57", "v58", "v59", "v60", "v61", "v62", "v63", "v64", "v65", "v66", "v67", "v68", "v69", "v70", "v71", "v72", "v73", "v74", "v75", "v76", "v77", "v78", "v79", "v80", "v81", "v82", "v83", "v84", "v85", "v86", "v87", "v88", "v89", "v90", "v91", "v92", "v93", "v94", "v95", "v96", "v97", "v98", "v99", "v100", "v101", "v102", "v103", "v104", "v105", "v106", "v107", "v108", "v109", "v110", "v111", "v112", "v113", "v114", "v115", "v116", "v117", "v118", "v119", "v120", "v121", "v122", "v123", "v124", "v125", "v126", "v127", "v128", "v129", "v130", "v131", "v132", "v133", "v134", "v135", "v136", "v137", "v138", "v139", "v140", "v141", "v142", "v143", "v144", "v145", "v146", "v147", "v148", "v149", "v150", "v151", "v152", "v153", "v154", "v155", "v156", "v157", "v158", "v159", "v232", "v233", "v234", "v235", "v236", "v237", "v238", "v239", "v240", "v241", "v242", "v243", "v244", "v245", "v246", "v247", "v248", "v249", "v250", "v251");
  const unsigned ex = __builtin_amdgcn_readfirstlane(xv);
  char* ws = (char*)uni64(wsp);
  char* out = (char*)uni64(outp);
  const rsrc_t rs = __builtin_amdgcn_make_buffer_rsrc(ws, 0, WS_END, 0x00020000);
  if (w >= 4) {
    p2_loader((unsigned long long)ws, out, ex, lane, w - 4, b);
    __builtin_amdgcn_endpgm();
  }
  p2_attn(m0, m1, m2, rs, ex, lane, w, b >> 3);
}

extern "C" void kernel_launch(void* const* d_in, const int* in_sizes, int n_in, void* d_out, int out_size, void* d_ws, size_t ws_size,
                              hipStream_t stream) {
  k_fused<<<256, 512, 0, stream>>>((const char*)d_in[0], (const char*)d_in[1], (const char*)d_in[2], (const char*)d_in[3],
                                   (const char*)d_in[4], (char*)d_ws, (char*)d_out);
}
```
